```python
import torch

def _softmax(x, axis=-1):
    return torch.softmax(x, dim=axis)

def _gelu(x):
    return torch.nn.functional.gelu(x, approximate='tanh')

def _silu(x):
    return torch.nn.functional.silu(x)

def _sigmoid(x):
    return torch.sigmoid(x)

def _layernorm(x, axis=-1, eps=1e-05):
    m = x.mean(dim=axis, keepdim=True)
    v = x.var(dim=axis, keepdim=True, unbiased=False)
    return (x - m) / torch.sqrt(v + eps)

def _scatter_add(tgt, idx, src, axis=0):
    return tgt.clone().index_add_(axis, idx, src)

def _segment_sum(src, seg, n):
    out = torch.zeros((n,) + src.shape[1:], dtype=src.dtype)
    return out.index_add_(0, seg, src)

def _pad(x, pw):
    flat = tuple((v for pair in reversed(pw) for v in pair))
    return torch.nn.functional.pad(x, flat)
kernel_name = 'sequential_preLN__memory_aug_recurrent_mem_tr__glu_gated_geglu__prompt'

def setup_inputs():

    _w = lambda a: (lambda t: t.to(torch.bfloat16) if t.is_floating_point() else t)(torch.as_tensor(a))

    def _A_setup():
        B, S, Dm = (1, 2048, 640)
        H, Dh = (32, 192)
        M, seg = (8, 32)
        seg = min(seg, S)
        S = S // seg * seg
        return {'x': _w((lambda *s: torch.randn(*s, dtype=torch.bfloat16) * 0.05)(B, S, Dm)), 'mem0': _w((lambda *s: torch.randn(*s, dtype=torch.bfloat16) * 0.2)(B, M, Dm)), 'Wq': _w((lambda *s: torch.randn(*s, dtype=torch.bfloat16) * 1.6)(Dm, H, Dh) * Dm ** (-0.5)), 'Wk': _w((lambda *s: torch.randn(*s, dtype=torch.bfloat16) * 1.6)(Dm, H, Dh) * Dm ** (-0.5)), 'Wv': _w((lambda *s: torch.randn(*s, dtype=torch.bfloat16) * 0.2)(Dm, H, Dh) * Dm ** (-0.5)), 'Wo': _w((lambda *s: torch.randn(*s, dtype=torch.bfloat16) * 0.2)(H, Dh, Dm) * (H * Dh) ** (-0.5)), 'seg_len': seg}
    _A_in = _A_setup()

    def _B_setup():
        B, S, Dm, Df = (1, 2048, 640, 1536)
        return {'x': _w((lambda *s: torch.randn(*s, dtype=torch.bfloat16) * 0.05)(B, S, Dm)), 'w_gate': _w((lambda *s: torch.randn(*s, dtype=torch.bfloat16) * 0.4)(Dm, Df) * Dm ** (-0.5)), 'w_up': _w((lambda *s: torch.randn(*s, dtype=torch.bfloat16) * 0.4)(Dm, Df) * Dm ** (-0.5)), 'w_down': _w((lambda *s: torch.randn(*s, dtype=torch.bfloat16) * 0.4)(Df, Dm) * Df ** (-0.5))}
    _B_in = _B_setup()
    return {'A_x': _A_in['x'], 'A_mem0': _A_in['mem0'], 'A_Wq': _A_in['Wq'], 'A_Wk': _A_in['Wk'], 'A_Wv': _A_in['Wv'], 'A_Wo': _A_in['Wo'], 'A_seg_len': _A_in['seg_len'], 'B_x': _B_in['x'], 'B_w_gate': _B_in['w_gate'], 'B_w_up': _B_in['w_up'], 'B_w_down': _B_in['w_down']}

def reference(A_x, A_mem0, A_Wq, A_Wk, A_Wv, A_Wo, A_seg_len, B_x, B_w_gate, B_w_up, B_w_down):

    def _A_ref(x, mem0, Wq, Wk, Wv, Wo, seg_len):
        B, S, Dm = x.shape
        H, Dh = (Wq.shape[1], Wq.shape[2])
        M = mem0.shape[1]
        scale = Dh ** (-0.5)
        n_seg = S // seg_len
        L = M + seg_len
        idx = torch.arange(L)
        mask = (idx[:, None] < M) | (idx[:, None] >= idx[None, :])
        mem = mem0
        outs = []
        for s in range(n_seg):
            xs = x[:, s * seg_len:(s + 1) * seg_len, :].clone()
            h = torch.concatenate([mem, xs], axis=1)
            q = torch.einsum('btd,dhe->bhte', h, Wq)
            k = torch.einsum('btd,dhe->bhte', h, Wk)
            v = torch.einsum('btd,dhe->bhte', h, Wv)
            scores = torch.einsum('bhqe,bhke->bhqk', q, k) * scale
            scores = torch.where(mask[None, None], scores, -1000000000.0)
            p = _softmax(scores, axis=-1)
            o = torch.einsum('bhqk,bhke->bhqe', p, v)
            o = torch.einsum('bhte,hed->btd', o, Wo)
            mem = o[:, :M, :].clone()
            outs.append(o[:, M:, :])
        return torch.concatenate(outs, axis=1)

    def _B_ref(x, w_gate, w_up, w_down):
        g = torch.einsum('bsd,df->bsf', x, w_gate)
        u = torch.einsum('bsd,df->bsf', x, w_up)
        h = _gelu(g) * u
        return torch.einsum('bsf,fd->bsd', h, w_down)
    x = A_x
    h = x + _A_ref(_layernorm(x, axis=-1), A_mem0, A_Wq, A_Wk, A_Wv, A_Wo, A_seg_len)
    h2 = h + _B_ref(_layernorm(h, axis=-1), B_w_gate, B_w_up, B_w_down)
    return h2
```

```cpp
#include <hip/hip_runtime.h>
#include <hip/hip_bf16.h>
#include <stdint.h>

constexpr int SEQ = 2048, DM = 640, NH = 32, DH = 192, NM = 8, SEG = 32, NSEG = 64, DF = 1536;
constexpr int KS_D = DM / 32;
constexpr int KS_F = DF / 32;
constexpr int NBK = 80;

constexpr size_t OFF_XNF  = 0;
constexpr size_t OFF_WQF  = OFF_XNF  + (size_t)128 * 20 * 1024;
constexpr size_t OFF_WKF  = OFF_WQF  + (size_t)384 * 20 * 1024;
constexpr size_t OFF_WVF  = OFF_WKF  + (size_t)384 * 20 * 1024;
constexpr size_t OFF_WOF  = OFF_WVF  + (size_t)384 * 20 * 1024;
constexpr size_t OFF_WGF  = OFF_WOF  + (size_t)32 * 40 * 6 * 1024;
constexpr size_t OFF_WUF  = OFF_WGF  + (size_t)96 * 20 * 1024;
constexpr size_t OFF_WDF  = OFF_WUF  + (size_t)96 * 20 * 1024;
constexpr size_t OFF_WVOF = OFF_WDF  + (size_t)40 * 48 * 1024;
constexpr size_t OFF_QX   = OFF_WVOF + (size_t)80 * 16 * 20 * 1024;
constexpr size_t OFF_KX   = OFF_QX   + (size_t)64 * 32 * 12288;
constexpr size_t OFF_UX   = OFF_KX   + (size_t)64 * 32 * 12288;
constexpr size_t OFF_MEM  = OFF_UX   + (size_t)64 * 80 * 16 * 1024;
constexpr size_t OFF_P    = OFF_MEM  + (size_t)65 * 10240;
constexpr size_t OFF_KM   = OFF_P    + (size_t)64 * 20480;
constexpr size_t OFF_UM   = OFF_KM   + (size_t)64 * 32 * 6144;
constexpr size_t OFF_H    = OFF_UM   + (size_t)64 * 32 * 10240;
constexpr size_t OFF_HNF  = OFF_H    + (size_t)2048 * 640 * 2;
constexpr size_t OFF_ACTF = OFF_HNF  + (size_t)128 * 20 * 1024;
constexpr size_t WS_TOTAL = OFF_ACTF + (size_t)128 * 48 * 1024;
static_assert(WS_TOTAL < (size_t)256 * 1024 * 1024, "ws too big");

typedef short bf16x8 __attribute__((ext_vector_type(8)));
typedef float f32x4 __attribute__((ext_vector_type(4)));
typedef unsigned v4u __attribute__((ext_vector_type(4)));
typedef unsigned v2u __attribute__((ext_vector_type(2)));
typedef unsigned short u16;

__device__ __forceinline__ f32x4 mfma(v4u a, v4u b, f32x4 c) {
  return __builtin_amdgcn_mfma_f32_16x16x32_bf16(__builtin_bit_cast(bf16x8, a), __builtin_bit_cast(bf16x8, b), c, 0, 0, 0);
}
typedef __bf16 bf16x2_t __attribute__((ext_vector_type(2)));
typedef float f32x2 __attribute__((ext_vector_type(2)));
__device__ __forceinline__ unsigned cvtpk(float lo, float hi) {
  f32x2 v = {lo, hi};
  return __builtin_bit_cast(unsigned, __builtin_convertvector(v, bf16x2_t));
}
__device__ __forceinline__ float bf2f(unsigned short b) { return __builtin_bit_cast(float, (unsigned)b << 16); }
__device__ __forceinline__ float bflo(unsigned u) { return __builtin_bit_cast(float, u << 16); }
__device__ __forceinline__ float bfhi(unsigned u) { return __builtin_bit_cast(float, u & 0xffff0000u); }
__device__ __forceinline__ float rbf(float x) { return bflo(cvtpk(x, x)); }
__device__ __forceinline__ v4u pack8(f32x4 a, f32x4 b) {
  v4u r; r.x = cvtpk(a.x, a.y); r.y = cvtpk(a.z, a.w); r.z = cvtpk(b.x, b.y); r.w = cvtpk(b.z, b.w); return r;
}
__device__ __forceinline__ v2u pack4(f32x4 a) { v2u r; r.x = cvtpk(a.x, a.y); r.y = cvtpk(a.z, a.w); return r; }
constexpr unsigned SENT = 0xFFFFFFFFu;
__device__ __forceinline__ unsigned desent(unsigned x) { return x == SENT ? 0x7FC07FC0u : x; }
__device__ __forceinline__ bool isbad(v4u v) { return v.x == SENT || v.y == SENT || v.z == SENT || v.w == SENT; }

#define RAW_BARRIER() do { asm volatile("s_waitcnt lgkmcnt(0)" ::: "memory"); __builtin_amdgcn_s_barrier(); } while (0)
#define WAIT_V(n) asm volatile("s_waitcnt vmcnt(" #n ")" ::: "memory")

__device__ __forceinline__ void glds16(const unsigned char* g, unsigned char* l) {
  __builtin_amdgcn_global_load_lds((const unsigned*)g, (unsigned*)l, 16, 0, 0);
}

struct PrepArgs {
  const u16* x; const u16* mem0; const u16* wq; const u16* wk; const u16* wv; const u16* wo;
  const u16* wg; const u16* wu; const u16* wd; unsigned char* ws;
};

__device__ __forceinline__ void relayout_tile(const u16* src, int ld, int k0, int n0, unsigned char* dst, int ks_per_nt,
                                              int ks_idx, int nt0, bool perm, u16* tile) {
  const int t = threadIdx.x;
#pragma unroll
  for (int i = 0; i < 2; ++i) {
    const int id = t + 256 * i, row = id >> 4, c8 = id & 15;
    const v4u v = *(const v4u*)(src + (size_t)(k0 + row) * ld + n0 + c8 * 8);
    *(v4u*)(tile + row * 136 + c8 * 8) = v;
  }
  __syncthreads();
#pragma unroll
  for (int i = 0; i < 2; ++i) {
    const int id = t + 256 * i, f = id >> 6, lane = id & 63, g = lane >> 4, n = lane & 15;
    unsigned w[4];
#pragma unroll
    for (int jj = 0; jj < 4; ++jj) {
      const int j0 = 2 * jj, j1 = 2 * jj + 1;
      const int ka = perm ? (16 * (j0 >> 2) + 4 * g + (j0 & 3)) : (8 * g + j0);
      const int kb = perm ? (16 * (j1 >> 2) + 4 * g + (j1 & 3)) : (8 * g + j1);
      w[jj] = (unsigned)tile[ka * 136 + 16 * f + n] | ((unsigned)tile[kb * 136 + 16 * f + n] << 16);
    }
    v4u o = {w[0], w[1], w[2], w[3]};
    *(v4u*)(dst + ((size_t)(nt0 + f) * ks_per_nt + ks_idx) * 1024 + lane * 16) = o;
  }
}

constexpr int PREP_LN = 128, PREP_WQKV = 960, PREP_WO = 960, PREP_WGU = 240, PREP_WD = 240;
constexpr int PREP_BLOCKS = PREP_LN + 3 * PREP_WQKV + PREP_WO + 2 * PREP_WGU + PREP_WD + 1;

__global__ void __launch_bounds__(256) k_prep(PrepArgs a) {
  __shared__ __attribute__((aligned(16))) u16 tile[32 * 136];
  int b = blockIdx.x;
  const int t = threadIdx.x;
  if (b < PREP_LN) {
    const int lane = t & 63, w = t >> 6, r = lane >> 4, c = lane & 15;
    const int row = b * 16 + w * 4 + r;
    v4u v[5];
    float s = 0.f;
#pragma unroll
    for (int i = 0; i < 5; ++i) {
      v[i] = *(const v4u*)(a.x + (size_t)row * DM + (c + 16 * i) * 8);
      s += bflo(v[i].x) + bfhi(v[i].x) + bflo(v[i].y) + bfhi(v[i].y) + bflo(v[i].z) + bfhi(v[i].z) + bflo(v[i].w) + bfhi(v[i].w);
    }
#pragma unroll
    for (int o = 1; o < 16; o <<= 1) s += __shfl_xor(s, o);
    const float mean = s * (1.f / DM);
    float q = 0.f;
#pragma unroll
    for (int i = 0; i < 5; ++i) {
      const unsigned u[4] = {v[i].x, v[i].y, v[i].z, v[i].w};
#pragma unroll
      for (int j = 0; j < 4; ++j) { float d0 = bflo(u[j]) - mean, d1 = bfhi(u[j]) - mean; q += d0 * d0 + d1 * d1; }
    }
#pragma unroll
    for (int o = 1; o < 16; o <<= 1) q += __shfl_xor(q, o);
    const float rs = rsqrtf(q * (1.f / DM) + 1e-5f);
#pragma unroll
    for (int i = 0; i < 5; ++i) {
      const unsigned u[4] = {v[i].x, v[i].y, v[i].z, v[i].w};
      v4u o;
      o.x = cvtpk((bflo(u[0]) - mean) * rs, (bfhi(u[0]) - mean) * rs);
      o.y = cvtpk((bflo(u[1]) - mean) * rs, (bfhi(u[1]) - mean) * rs);
      o.z = cvtpk((bflo(u[2]) - mean) * rs, (bfhi(u[2]) - mean) * rs);
      o.w = cvtpk((bflo(u[3]) - mean) * rs, (bfhi(u[3]) - mean) * rs);
      const int ch = c + 16 * i;
      *(v4u*)(a.ws + OFF_XNF + ((size_t)(row >> 4) * KS_D + (ch >> 2)) * 1024 + ((ch & 3) * 16 + (row & 15)) * 16) = o;
    }
    return;
  }
  b -= PREP_LN;
  if (b < 3 * PREP_WQKV) {
    const int which = b / PREP_WQKV, r = b % PREP_WQKV, ks = r % 20, nc = r / 20;
    const u16* src = which == 0 ? a.wq : (which == 1 ? a.wk : a.wv);
    unsigned char* dst = a.ws + (which == 0 ? OFF_WQF : (which == 1 ? OFF_WKF : OFF_WVF));
    relayout_tile(src, NH * DH, ks * 32, nc * 128, dst, KS_D, ks, nc * 8, false, tile);
    return;
  }
  b -= 3 * PREP_WQKV;
  if (b < PREP_WO) {
    const int kk = b % 192, nc = b / 192, h = kk / 6, pr = kk % 6;
    relayout_tile(a.wo, DM, kk * 32, nc * 128, a.ws + OFF_WOF + (size_t)h * 40 * 6 * 1024, 6, pr, nc * 8, true, tile);
    return;
  }
  b -= PREP_WO;
  if (b < 2 * PREP_WGU) {
    const int which = b / PREP_WGU, r = b % PREP_WGU, ks = r % 20, nc = r / 20;
    relayout_tile(which ? a.wu : a.wg, DF, ks * 32, nc * 128, a.ws + (which ? OFF_WUF : OFF_WGF), KS_D, ks, nc * 8, true, tile);
    return;
  }
  b -= 2 * PREP_WGU;
  if (b < PREP_WD) {
    const int ks = b % 48, nc = b / 48;
    relayout_tile(a.wd, DM, ks * 32, nc * 128, a.ws + OFF_WDF, KS_F, ks, nc * 8, true, tile);
    return;
  }
  for (int id = t; id < 640; id += 256) {
    const int nb = id >> 3, tok = id & 7;
    v4u v = *(const v4u*)(a.mem0 + tok * DM + nb * 8);
    v.x = desent(v.x); v.y = desent(v.y); v.z = desent(v.z); v.w = desent(v.w);
    *(v4u*)(a.ws + OFF_MEM + id * 16) = v;
  }
}

__global__ void __launch_bounds__(256) k_wvo(const u16* wv, unsigned char* ws) {
  const int lane = threadIdx.x & 63, w = threadIdx.x >> 6, g = lane >> 4, l16 = lane & 15;
  const int wid = blockIdx.x * 4 + w;
  const int h = wid / 10, a4 = wid % 10;
  v4u af[4][6];
#pragma unroll
  for (int i = 0; i < 4; ++i)
#pragma unroll
    for (int p = 0; p < 6; ++p) {
      const u16* src = wv + (size_t)((a4 * 4 + i) * 16 + l16) * (NH * DH) + h * DH + 32 * p + 4 * g;
      const v2u lo = *(const v2u*)src, hi = *(const v2u*)(src + 16);
      af[i][p] = v4u{lo.x, lo.y, hi.x, hi.y};
    }
  const unsigned char* wo = ws + OFF_WOF + (size_t)h * 40 * 6 * 1024 + lane * 16;
  for (int nt = 0; nt < 40; ++nt) {
    v4u bf[6];
#pragma unroll
    for (int p = 0; p < 6; ++p) bf[p] = *(const v4u*)(wo + (size_t)(nt * 6 + p) * 1024);
#pragma unroll
    for (int i = 0; i < 4; ++i) {
      f32x4 acc = {0.f, 0.f, 0.f, 0.f};
#pragma unroll
      for (int p = 0; p < 6; ++p) acc = mfma(af[i][p], bf[p], acc);
      const int dt = a4 * 4 + i, ks = dt >> 1, gp = (dt & 1) * 2 + (g >> 1);
      const int nb = 2 * nt + (l16 >> 3);
      unsigned char* dst = ws + OFF_WVOF + ((size_t)(nb * 16 + (h >> 1)) * 20 + ks) * 1024 + (gp * 16 + (h & 1) * 8 + (l16 & 7)) * 16 + (g & 1) * 8;
      *(v2u*)dst = pack4(acc);
    }
  }
}

template <int NA, int KS, int KPS, class FA>
__device__ __forceinline__ void gemm_loop(unsigned char* smem, const unsigned char* wsb, unsigned boff  , int b_ks_total,
                                          FA afrag, f32x4 (&acc)[NA][2]) {
  constexpr int FPK = NA + 8, FPS = FPK * KPS, LPW = FPS / 4, NST = KS / KPS, BUFB = FPS * 1024;
  static_assert(FPS % 4 == 0 && KS % KPS == 0, "");
  const int lane = threadIdx.x & 63, w = __builtin_amdgcn_readfirstlane(threadIdx.x >> 6);
  unsigned soff[LPW];
#pragma unroll
  for (int i = 0; i < LPW; ++i) {
    const int f = w * LPW + i, kk = f / FPK, ff = f % FPK;
    const unsigned o = ff < NA ? afrag(ff) : boff + (unsigned)(ff - NA) * (unsigned)b_ks_total * 1024u;
    soff[i] = __builtin_amdgcn_readfirstlane(o + kk * 1024u);
  }
  const unsigned char* gl = wsb + lane * 16;
  auto stage = [&](int st, int buf) {
#pragma unroll
    for (int i = 0; i < LPW; ++i)
      glds16(gl + (soff[i] + (unsigned)st * (KPS * 1024u)), smem + buf * BUFB + (w * LPW + i) * 1024);
  };
  stage(0, 0);
  if (NST > 1) stage(1, 1);
  for (int st = 0; st < NST; ++st) {
    if (st + 1 < NST) { asm volatile("s_waitcnt vmcnt(%0)" ::"n"(LPW) : "memory"); } else { WAIT_V(0); }
    RAW_BARRIER();
    if (st + 2 < NST) stage(st + 2, (st + 2) % 3);
    const unsigned char* buf = smem + (st % 3) * BUFB + lane * 16;
#pragma unroll
    for (int kk = 0; kk < KPS; ++kk) {
      const v4u b0 = *(const v4u*)(buf + (kk * FPK + NA + 2 * w) * 1024);
      const v4u b1 = *(const v4u*)(buf + (kk * FPK + NA + 2 * w + 1) * 1024);
#pragma unroll
      for (int a = 0; a < NA; ++a) {
        const v4u af = *(const v4u*)(buf + (kk * FPK + a) * 1024);
        acc[a][0] = mfma(af, b0, acc[a][0]);
        acc[a][1] = mfma(af, b1, acc[a][1]);
      }
    }
  }
}

__global__ void __launch_bounds__(256, 1) k_qkvu(unsigned char* ws) {
  extern __shared__ __attribute__((aligned(16))) unsigned char smem[];
  const int m = blockIdx.x >> 5, h = blockIdx.x & 31;
  const int lane = threadIdx.x & 63, w = __builtin_amdgcn_readfirstlane(threadIdx.x >> 6), g = lane >> 4, l16 = lane & 15;
  f32x4 acc[36][2];
#pragma unroll
  for (int a = 0; a < 36; ++a) { acc[a][0] = f32x4{0, 0, 0, 0}; acc[a][1] = f32x4{0, 0, 0, 0}; }
  auto afrag = [&](int ff) -> unsigned {
    const unsigned base = ff < 12 ? (unsigned)OFF_WQF : (ff < 24 ? (unsigned)OFF_WKF : (unsigned)OFF_WVF);
    const int et = ff < 12 ? ff : (ff < 24 ? ff - 12 : ff - 24);
    return base + (unsigned)(h * 12 + et) * KS_D * 1024u;
  };
  gemm_loop<36, KS_D, 1>(smem, ws, (unsigned)OFF_XNF + (unsigned)m * 8 * KS_D * 1024u, KS_D, afrag, acc);
  const int seg = m * 4 + w;
#pragma unroll
  for (int which = 0; which < 2; ++which) {
    unsigned char* base = ws + (which ? OFF_KX : OFF_QX) + (size_t)(seg * 32 + h) * 12288;
#pragma unroll
    for (int T = 0; T < 2; ++T)
#pragma unroll
      for (int gr = 0; gr < 4; ++gr) {
        unsigned char* p = base + (T * 4 + gr) * 1536;
        *(v4u*)(p + lane * 16) = pack8(acc[which * 12 + 3 * gr][T], acc[which * 12 + 3 * gr + 1][T]);
        *(v2u*)(p + 1024 + lane * 8) = pack4(acc[which * 12 + 3 * gr + 2][T]);
      }
  }
  v4u vf[2][6];
#pragma unroll
  for (int T = 0; T < 2; ++T)
#pragma unroll
    for (int p = 0; p < 6; ++p) vf[T][p] = pack8(acc[24 + 2 * p][T], acc[24 + 2 * p + 1][T]);
  const unsigned char* wo = ws + OFF_WOF + (size_t)h * 40 * 6 * 1024;
  auto stage2 = [&](int st, int buf) {
#pragma unroll
    for (int i = 0; i < 6; ++i) {
      const int f = w * 6 + i;
      glds16(wo + (size_t)(st * 24 + f) * 1024 + lane * 16, smem + buf * 24576 + f * 1024);
    }
  };
  WAIT_V(0);
  RAW_BARRIER();
  stage2(0, 0); stage2(1, 1);
  for (int st = 0; st < 10; ++st) {
    if (st == 0) { WAIT_V(6); } else if (st + 1 < 10) { WAIT_V(10); } else { WAIT_V(4); }
    RAW_BARRIER();
    if (st + 2 < 10) stage2(st + 2, (st + 2) % 3);
    const unsigned char* buf = smem + (st % 3) * 24576 + lane * 16;
#pragma unroll
    for (int i = 0; i < 4; ++i) {
      f32x4 d0 = {0, 0, 0, 0}, d1 = {0, 0, 0, 0};
#pragma unroll
      for (int p = 0; p < 6; ++p) {
        const v4u bf = *(const v4u*)(buf + (i * 6 + p) * 1024);
        d0 = mfma(vf[0][p], bf, d0);
        d1 = mfma(vf[1][p], bf, d1);
      }
      const int nt = st * 4 + i, nb = 2 * nt + (l16 >> 3);
      *(v4u*)(ws + OFF_UX + ((size_t)(seg * 80 + nb) * 16 + (h >> 1)) * 1024 + (g * 16 + (h & 1) * 8 + (l16 & 7)) * 16) = pack8(d0, d1);
    }
  }
}

constexpr int QK_NREG = 104;
constexpr int QK_NLDS = 120 - QK_NREG;
constexpr unsigned SPIN_CAP = 1u << 22;

__device__ __forceinline__ v4u ld_sc1(__amdgpu_buffer_rsrc_t rs, int off) { return __builtin_amdgcn_raw_buffer_load_b128(rs, off, 0, 16); }

__device__ __forceinline__ void fetch_mem(__amdgpu_buffer_rsrc_t rs, int s, unsigned char* lmem) {
  const int tid = threadIdx.x;
  const int base = (int)OFF_MEM + s * 10240;
  const int o0 = base + tid * 16, o1 = base + (tid + 256) * 16, o2 = base + ((tid & 127) + 512) * 16;
  v4u m0, m1, m2;
  for (unsigned spins = 0; spins < SPIN_CAP; ++spins) {
    asm volatile("" ::: "memory");
    m0 = ld_sc1(rs, o0); m1 = ld_sc1(rs, o1); m2 = ld_sc1(rs, o2);
    const bool ok = !isbad(m0) && !isbad(m1) && !isbad(m2);
    if (__all(ok)) break;
  }
  *(v4u*)(lmem + tid * 16) = m0;
  *(v4u*)(lmem + (tid + 256) * 16) = m1;
  *(v4u*)(lmem + ((tid & 127) + 512) * 16) = m2;
}

__global__ void __launch_bounds__(256, 1) k_chain(unsigned char* ws) {
  extern __shared__ __attribute__((aligned(16))) unsigned char smem[];
  const __amdgpu_buffer_rsrc_t rs = __builtin_amdgcn_make_buffer_rsrc((void*)ws, 0, (int)WS_TOTAL, 0x00020000);
  const int tid = threadIdx.x, lane = tid & 63, w = __builtin_amdgcn_readfirstlane(tid >> 6), g = lane >> 4, l16 = lane & 15;
  unsigned char* lmem = smem;
  float* red = (float*)(smem + 10240);
  unsigned char* lw = smem + 10240 + 12288;
  const int bfo = g * 128 + (l16 & 7) * 16;
  if (blockIdx.x < NH) {
    const int h = blockIdx.x;
    v4u wr[QK_NREG];
    auto wsrc = [&](int f) -> const unsigned char* {
      const int ks = f / 6, tl = f % 6;
      return ws + (tl < 3 ? OFF_WQF : OFF_WKF) + ((size_t)(h * 12 + 3 * w + (tl % 3)) * KS_D + ks) * 1024 + lane * 16;
    };
#pragma unroll
    for (int f = 0; f < QK_NREG; ++f) wr[f] = *(const v4u*)wsrc(f);
#pragma unroll
    for (int f = 0; f < QK_NLDS; ++f) *(v4u*)(lw + (w * QK_NLDS + f) * 1024 + lane * 16) = *(const v4u*)wsrc(QK_NREG + f);
    const float scale = 0.07216878364870322f;
    for (int s = 0; s < NSEG; ++s) {
      const unsigned char* kx = ws + OFF_KX + (size_t)(s * 32 + h) * 12288 + w * 1536;
      const v4u kp0 = *(const v4u*)(kx + lane * 16), kp1 = *(const v4u*)(kx + 4 * 1536 + lane * 16);
      const v2u ks0 = *(const v2u*)(kx + 1024 + lane * 8), ks1 = *(const v2u*)(kx + 4 * 1536 + 1024 + lane * 8);
      fetch_mem(rs, s, lmem);
      __syncthreads();
      f32x4 acc[6];
#pragma unroll
      for (int t = 0; t < 6; ++t) acc[t] = f32x4{0, 0, 0, 0};
#pragma unroll
      for (int ks = 0; ks < KS_D; ++ks) {
        const v4u b = *(const v4u*)(lmem + ks * 512 + bfo);
#pragma unroll
        for (int t = 0; t < 6; ++t) {
          const int f = ks * 6 + t;
          const v4u a = f < QK_NREG ? wr[f < QK_NREG ? f : 0] : *(const v4u*)(lw + (w * QK_NLDS + (f - QK_NREG)) * 1024 + lane * 16);
          acc[t] = mfma(a, b, acc[t]);
        }
      }
      const v4u qp = pack8(acc[0], acc[1]);
      const v2u qs2 = pack4(acc[2]);
      const v4u qs = {qs2.x, qs2.y, 0u, 0u};
      const v4u kmp = pack8(acc[3], acc[4]);
      const v2u kms2 = pack4(acc[5]);
      const v4u kms = {kms2.x, kms2.y, 0u, 0u};
      f32x4 z = {0, 0, 0, 0};
      f32x4 s0 = mfma(kmp, qp, z); s0 = mfma(kms, qs, s0);
      f32x4 s1 = mfma(kp0, qp, z); s1 = mfma(v4u{ks0.x, ks0.y, 0u, 0u}, qs, s1);
      f32x4 s2 = mfma(kp1, qp, z); s2 = mfma(v4u{ks1.x, ks1.y, 0u, 0u}, qs, s2);
      *(f32x4*)(red + ((w * 3 + 0) * 64 + lane) * 4) = s0;
      *(f32x4*)(red + ((w * 3 + 1) * 64 + lane) * 4) = s1;
      *(f32x4*)(red + ((w * 3 + 2) * 64 + lane) * 4) = s2;
      __syncthreads();
      if (w == 0) {
        f32x4 t0 = z, t1 = z, t2 = z;
#pragma unroll
        for (int ww = 0; ww < 4; ++ww) {
          t0 += *(const f32x4*)(red + ((ww * 3 + 0) * 64 + lane) * 4);
          t1 += *(const f32x4*)(red + ((ww * 3 + 1) * 64 + lane) * 4);
          t2 += *(const f32x4*)(red + ((ww * 3 + 2) * 64 + lane) * 4);
        }
        t0 *= scale; t1 *= scale; t2 *= scale;
        const bool mv = g < 2;
        float mx = fmaxf(fmaxf(fmaxf(t1.x, t1.y), fmaxf(t1.z, t1.w)), fmaxf(fmaxf(t2.x, t2.y), fmaxf(t2.z, t2.w)));
        if (mv) mx = fmaxf(mx, fmaxf(fmaxf(t0.x, t0.y), fmaxf(t0.z, t0.w)));
        mx = fmaxf(mx, __shfl_xor(mx, 16));
        mx = fmaxf(mx, __shfl_xor(mx, 32));
        f32x4 e0, e1, e2;
        e0.x = mv ? __expf(t0.x - mx) : 0.f; e0.y = mv ? __expf(t0.y - mx) : 0.f; e0.z = mv ? __expf(t0.z - mx) : 0.f; e0.w = mv ? __expf(t0.w - mx) : 0.f;
        e1.x = __expf(t1.x - mx); e1.y = __expf(t1.y - mx); e1.z = __expf(t1.z - mx); e1.w = __expf(t1.w - mx);
        e2.x = __expf(t2.x - mx); e2.y = __expf(t2.y - mx); e2.z = __expf(t2.z - mx); e2.w = __expf(t2.w - mx);
        float sum = (e0.x + e0.y + e0.z + e0.w) + (e1.x + e1.y + e1.z + e1.w) + (e2.x + e2.y + e2.z + e2.w);
        sum += __shfl_xor(sum, 16);
        sum += __shfl_xor(sum, 32);
        const float inv = 1.f / sum;
        e0 *= inv; e1 *= inv; e2 *= inv;
        v4u px = pack8(e1, e2);
        v2u pm = pack4(e0);
        px.x = desent(px.x); px.y = desent(px.y); px.z = desent(px.z); px.w = desent(px.w);
        pm.x = desent(pm.x); pm.y = desent(pm.y);
        const int pb = (int)OFF_P + s * 20480 + (h >> 3) * 5120;
        if (l16 < 8) {
          __builtin_amdgcn_raw_buffer_store_b128(px, rs, pb + ((h >> 1) & 3) * 1024 + (g * 16 + (h & 1) * 8 + l16) * 16, 0, 16);
          if (mv) __builtin_amdgcn_raw_buffer_store_b64(pm, rs, pb + 4096 + (((h >> 1) & 3) * 16 + (h & 1) * 8 + l16) * 16 + g * 8, 0, 16);
        }
      }
      unsigned char* km = ws + OFF_KM + (size_t)(s * 32 + h) * 6144 + w * 1536;
      *(v4u*)(km + lane * 16) = kmp;
      *(v2u*)(km + 1024 + lane * 8) = kms2;
    }
  } else if (blockIdx.x < NH + NBK) {
    const int nb = blockIdx.x - NH;
    v4u wr[80];
#pragma unroll
    for (int f = 0; f < 80; ++f)
      wr[f] = *(const v4u*)(ws + OFF_WVOF + ((size_t)(nb * 16 + 4 * w + f / 20) * 20 + f % 20) * 1024 + lane * 16);
    unsigned char* lu = lw + w * 1024;
    for (int s = 0; s < NSEG; ++s) {
      v4u ux[4];
#pragma unroll
      for (int t = 0; t < 4; ++t) ux[t] = *(const v4u*)(ws + OFF_UX + ((size_t)(s * 80 + nb) * 16 + 4 * w + t) * 1024 + lane * 16);
      fetch_mem(rs, s, lmem);
      __syncthreads();
      f32x4 acc[4];
#pragma unroll
      for (int t = 0; t < 4; ++t) acc[t] = f32x4{0, 0, 0, 0};
#pragma unroll
      for (int ks = 0; ks < KS_D; ++ks) {
        const v4u a = *(const v4u*)(lmem + ks * 512 + bfo);
#pragma unroll
        for (int t = 0; t < 4; ++t) acc[t] = mfma(a, wr[t * 20 + ks], acc[t]);
      }
      if (g < 2) {
#pragma unroll
        for (int t = 0; t < 4; ++t) *(v2u*)(lu + (t * 16 + l16) * 16 + g * 8) = pack4(acc[t]);
      }
      asm volatile("s_waitcnt lgkmcnt(0)" ::: "memory");
      const v4u um = *(const v4u*)(lu + (g * 16 + l16) * 16);
      *(v4u*)(ws + OFF_UM + ((size_t)(s * 32 + 8 * w + 2 * g + (l16 >> 3)) * 640 + 8 * nb + (l16 & 7)) * 16) = um;
      const int pb = (int)OFF_P + s * 20480 + w * 5120 + lane * 16;
      v4u p0, p1, p2, p3, p4;
      for (unsigned spins = 0; spins < SPIN_CAP; ++spins) {
        asm volatile("" ::: "memory");
        p0 = ld_sc1(rs, pb); p1 = ld_sc1(rs, pb + 1024); p2 = ld_sc1(rs, pb + 2048); p3 = ld_sc1(rs, pb + 3072); p4 = ld_sc1(rs, pb + 4096);
        const bool ok = !isbad(p0) && !isbad(p1) && !isbad(p2) && !isbad(p3) && !isbad(p4);
        if (__all(ok)) break;
      }
      f32x4 d = {0, 0, 0, 0};
      d = mfma(ux[0], p0, d); d = mfma(ux[1], p1, d); d = mfma(ux[2], p2, d); d = mfma(ux[3], p3, d); d = mfma(um, p4, d);
      if ((l16 < 8) == (g < 2)) *(f32x4*)(red + (((w * 2 + (g >> 1)) * 8 + (l16 & 7)) * 8 + (g & 1) * 4)) = d;
      __syncthreads();
      if (tid < 8) {
        f32x4 a0 = {0, 0, 0, 0}, a1 = {0, 0, 0, 0};
#pragma unroll
        for (int q = 0; q < 8; ++q) {
          a0 += *(const f32x4*)(red + (q * 8 + tid) * 8);
          a1 += *(const f32x4*)(red + (q * 8 + tid) * 8 + 4);
        }
        v4u o = pack8(a0, a1);
        o.x = desent(o.x); o.y = desent(o.y); o.z = desent(o.z); o.w = desent(o.w);
        __builtin_amdgcn_raw_buffer_store_b128(o, rs, (int)OFF_MEM + (s + 1) * 10240 + nb * 128 + tid * 16, 0, 16);
      }
    }
  }
}

__global__ void __launch_bounds__(256, 1) k_post(const u16* x, unsigned char* ws) {
  extern __shared__ __attribute__((aligned(16))) unsigned char smem[];
  const int seg = blockIdx.x;
  const int tid = threadIdx.x, lane = tid & 63, w = __builtin_amdgcn_readfirstlane(tid >> 6), g = lane >> 4, l16 = lane & 15;
  unsigned char* lpx = smem;
  unsigned char* lpm = smem + 65536;
  float* lred = (float*)(smem + 65536 + 16384);
  const float scale = 0.07216878364870322f;
  for (int hh = 0; hh < 8; ++hh) {
    const int h = 8 * w + hh;
    const unsigned char* qb = ws + OFF_QX + (size_t)(seg * 32 + h) * 12288;
    const unsigned char* kb = ws + OFF_KX + (size_t)(seg * 32 + h) * 12288;
    const unsigned char* mb = ws + OFF_KM + (size_t)(seg * 32 + h) * 6144;
    v4u q[2][6], k[3][6];
    auto loadf = [&](const unsigned char* base, v4u (&o)[6]) {
#pragma unroll
      for (int gr = 0; gr < 4; ++gr) o[gr] = *(const v4u*)(base + gr * 1536 + lane * 16);
#pragma unroll
      for (int pr = 0; pr < 2; ++pr) {
        const v2u a = *(const v2u*)(base + (2 * pr) * 1536 + 1024 + lane * 8), b = *(const v2u*)(base + (2 * pr + 1) * 1536 + 1024 + lane * 8);
        o[4 + pr] = v4u{a.x, a.y, b.x, b.y};
      }
    };
    loadf(qb, q[0]); loadf(qb + 6144, q[1]); loadf(mb, k[0]); loadf(kb, k[1]); loadf(kb + 6144, k[2]);
#pragma unroll
    for (int T = 0; T < 2; ++T) {
      f32x4 t[3];
#pragma unroll
      for (int kt = 0; kt < 3; ++kt) {
        t[kt] = f32x4{0, 0, 0, 0};
#pragma unroll
        for (int f = 0; f < 6; ++f) t[kt] = mfma(k[kt][f], q[T][f], t[kt]);
        t[kt] *= scale;
      }
      const int tq = 16 * T + l16;
      const float NEG = -1e30f;
      float v[12];
#pragma unroll
      for (int r = 0; r < 4; ++r) {
        v[r] = g < 2 ? t[0][r] : NEG;
        v[4 + r] = (4 * g + r <= tq) ? t[1][r] : NEG;
        v[8 + r] = (16 + 4 * g + r <= tq) ? t[2][r] : NEG;
      }
      float mx = v[0];
#pragma unroll
      for (int i = 1; i < 12; ++i) mx = fmaxf(mx, v[i]);
      mx = fmaxf(mx, __shfl_xor(mx, 16));
      mx = fmaxf(mx, __shfl_xor(mx, 32));
      float sum = 0.f;
#pragma unroll
      for (int i = 0; i < 12; ++i) { v[i] = __expf(v[i] - mx); sum += v[i]; }
      sum += __shfl_xor(sum, 16);
      sum += __shfl_xor(sum, 32);
      const float inv = 1.f / sum;
      v4u px = {cvtpk(v[4] * inv, v[5] * inv), cvtpk(v[6] * inv, v[7] * inv), cvtpk(v[8] * inv, v[9] * inv), cvtpk(v[10] * inv, v[11] * inv)};
      *(v4u*)(lpx + (h * 2 + T) * 1024 + lane * 16) = px;
      if (g < 2) {
        v2u pm = {cvtpk(v[0] * inv, v[1] * inv), cvtpk(v[2] * inv, v[3] * inv)};
        *(v2u*)(lpm + ((h >> 2) * 2 + T) * 1024 + ((h & 3) * 16 + l16) * 16 + g * 8) = pm;
      }
    }
  }
  __syncthreads();
  f32x4 acc[10][2];
#pragma unroll
  for (int c = 0; c < 10; ++c) { acc[c][0] = f32x4{0, 0, 0, 0}; acc[c][1] = f32x4{0, 0, 0, 0}; }
  for (int h = 0; h < 32; ++h) {
    const v4u b0 = *(const v4u*)(lpx + (h * 2) * 1024 + lane * 16), b1 = *(const v4u*)(lpx + (h * 2 + 1) * 1024 + lane * 16);
#pragma unroll
    for (int c = 0; c < 10; ++c) {
      const int ct = 10 * w + c;
      const v4u a = *(const v4u*)(ws + OFF_UX + ((size_t)(seg * 80 + 2 * ct + (l16 >> 3)) * 16 + (h >> 1)) * 1024 + (g * 16 + (h & 1) * 8 + (l16 & 7)) * 16);
      acc[c][0] = mfma(a, b0, acc[c][0]);
      acc[c][1] = mfma(a, b1, acc[c][1]);
    }
  }
  for (int hq = 0; hq < 8; ++hq) {
    const v4u b0 = *(const v4u*)(lpm + (hq * 2) * 1024 + lane * 16), b1 = *(const v4u*)(lpm + (hq * 2 + 1) * 1024 + lane * 16);
#pragma unroll
    for (int c = 0; c < 10; ++c) {
      const int ct = 10 * w + c;
      const v4u a = *(const v4u*)(ws + OFF_UM + ((size_t)(seg * 32 + 4 * hq + g) * 640 + 16 * ct + l16) * 16);
      acc[c][0] = mfma(a, b0, acc[c][0]);
      acc[c][1] = mfma(a, b1, acc[c][1]);
    }
  }
  float sum[2] = {0.f, 0.f};
#pragma unroll
  for (int T = 0; T < 2; ++T) {
    const int tok = seg * 32 + 16 * T + l16;
#pragma unroll
    for (int c = 0; c < 10; ++c) {
      const int n = 16 * (10 * w + c) + 4 * g;
      const v2u xv = *(const v2u*)(x + (size_t)tok * DM + n);
      f32x4 hv;
      hv.x = rbf(bflo(xv.x) + rbf(acc[c][T].x)); hv.y = rbf(bfhi(xv.x) + rbf(acc[c][T].y));
      hv.z = rbf(bflo(xv.y) + rbf(acc[c][T].z)); hv.w = rbf(bfhi(xv.y) + rbf(acc[c][T].w));
      acc[c][T] = hv;
      *(v2u*)(ws + OFF_H + ((size_t)tok * DM + n) * 2) = pack4(hv);
      sum[T] += (hv.x + hv.y) + (hv.z + hv.w);
    }
    sum[T] += __shfl_xor(sum[T], 16);
    sum[T] += __shfl_xor(sum[T], 32);
    if (g == 0) lred[w * 32 + 16 * T + l16] = sum[T];
  }
  __syncthreads();
  float mean[2], var[2];
#pragma unroll
  for (int T = 0; T < 2; ++T) {
    mean[T] = (lred[16 * T + l16] + lred[32 + 16 * T + l16] + lred[64 + 16 * T + l16] + lred[96 + 16 * T + l16]) * (1.f / DM);
    float q = 0.f;
#pragma unroll
    for (int c = 0; c < 10; ++c) {
      f32x4 d = acc[c][T] - mean[T];
      q += d.x * d.x + d.y * d.y + d.z * d.z + d.w * d.w;
    }
    q += __shfl_xor(q, 16);
    q += __shfl_xor(q, 32);
    var[T] = q;
  }
  __syncthreads();
#pragma unroll
  for (int T = 0; T < 2; ++T) if (g == 0) lred[w * 32 + 16 * T + l16] = var[T];
  __syncthreads();
#pragma unroll
  for (int T = 0; T < 2; ++T) {
    const float vv = (lred[16 * T + l16] + lred[32 + 16 * T + l16] + lred[64 + 16 * T + l16] + lred[96 + 16 * T + l16]) * (1.f / DM);
    const float rs = rsqrtf(vv + 1e-5f);
#pragma unroll
    for (int i = 0; i < 5; ++i) {
      const f32x4 a = (acc[2 * i][T] - mean[T]) * rs, b = (acc[2 * i + 1][T] - mean[T]) * rs;
      *(v4u*)(ws + OFF_HNF + ((size_t)(seg * 2 + T) * KS_D + 5 * w + i) * 1024 + lane * 16) = pack8(a, b);
    }
  }
}

__device__ __forceinline__ float gelu_tanh(float x) {
  const float u = 0.7978845608028654f * (x + 0.044715f * x * x * x);
  const float e = __expf(2.f * u);
  const float t = 1.f - 2.f / (1.f + e);
  return 0.5f * x * (1.f + t);
}

__global__ void __launch_bounds__(256, 1) k_ffn1(unsigned char* ws) {
  extern __shared__ __attribute__((aligned(16))) unsigned char smem[];
  const int m = blockIdx.x >> 3, part = blockIdx.x & 7;
  const int lane = threadIdx.x & 63, w = __builtin_amdgcn_readfirstlane(threadIdx.x >> 6);
  f32x4 acc[24][2];
#pragma unroll
  for (int a = 0; a < 24; ++a) { acc[a][0] = f32x4{0, 0, 0, 0}; acc[a][1] = f32x4{0, 0, 0, 0}; }
  auto afrag = [&](int ff) -> unsigned {
    return (ff < 12 ? (unsigned)OFF_WGF : (unsigned)OFF_WUF) + (unsigned)(part * 12 + (ff < 12 ? ff : ff - 12)) * KS_D * 1024u;
  };
  gemm_loop<24, KS_D, 1>(smem, ws, (unsigned)OFF_HNF + (unsigned)m * 8 * KS_D * 1024u, KS_D, afrag, acc);
#pragma unroll
  for (int T = 0; T < 2; ++T)
#pragma unroll
    for (int p = 0; p < 6; ++p) {
      f32x4 o[2];
#pragma unroll
      for (int i = 0; i < 2; ++i)
#pragma unroll
        for (int r = 0; r < 4; ++r) {
          const float gg = rbf(acc[2 * p + i][T][r]), uu = rbf(acc[12 + 2 * p + i][T][r]);
          o[i][r] = rbf(gelu_tanh(gg)) * uu;
        }
      *(v4u*)(ws + OFF_ACTF + ((size_t)(m * 8 + 2 * w + T) * KS_F + part * 6 + p) * 1024 + lane * 16) = pack8(o[0], o[1]);
    }
}

__global__ void __launch_bounds__(256, 1) k_ffn2(unsigned char* ws, u16* out) {
  extern __shared__ __attribute__((aligned(16))) unsigned char smem[];
  const int m = blockIdx.x >> 2, part = blockIdx.x & 3;
  const int lane = threadIdx.x & 63, w = __builtin_amdgcn_readfirstlane(threadIdx.x >> 6), g = lane >> 4, l16 = lane & 15;
  f32x4 acc[10][2];
#pragma unroll
  for (int a = 0; a < 10; ++a) { acc[a][0] = f32x4{0, 0, 0, 0}; acc[a][1] = f32x4{0, 0, 0, 0}; }
  auto afrag = [&](int ff) -> unsigned { return (unsigned)OFF_WDF + (unsigned)(part * 10 + ff) * KS_F * 1024u; };
  gemm_loop<10, KS_F, 2>(smem, ws, (unsigned)OFF_ACTF + (unsigned)m * 8 * KS_F * 1024u, KS_F, afrag, acc);
#pragma unroll
  for (int T = 0; T < 2; ++T) {
    const int tok = m * 128 + 32 * w + 16 * T + l16;
#pragma unroll
    for (int c = 0; c < 10; ++c) {
      const int n = 16 * (part * 10 + c) + 4 * g;
      const v2u hv = *(const v2u*)(ws + OFF_H + ((size_t)tok * DM + n) * 2);
      f32x4 o;
      o.x = bflo(hv.x) + rbf(acc[c][T].x); o.y = bfhi(hv.x) + rbf(acc[c][T].y);
      o.z = bflo(hv.y) + rbf(acc[c][T].z); o.w = bfhi(hv.y) + rbf(acc[c][T].w);
      *(v2u*)(out + (size_t)tok * DM + n) = pack4(o);
    }
  }
}

extern "C" void kernel_launch(void* const* d_in, const int* in_sizes, int n_in, void* d_out, int out_size, void* d_ws,
                              size_t ws_size, hipStream_t stream) {
  unsigned char* ws = (unsigned char*)d_ws;
  PrepArgs pa;
  pa.x = (const u16*)d_in[0]; pa.mem0 = (const u16*)d_in[1]; pa.wq = (const u16*)d_in[2]; pa.wk = (const u16*)d_in[3];
  pa.wv = (const u16*)d_in[4]; pa.wo = (const u16*)d_in[5]; pa.wg = (const u16*)d_in[8]; pa.wu = (const u16*)d_in[9];
  pa.wd = (const u16*)d_in[10]; pa.ws = ws;
  (void)hipFuncSetAttribute((const void*)k_qkvu, hipFuncAttributeMaxDynamicSharedMemorySize, 3 * 44 * 1024);
  (void)hipFuncSetAttribute((const void*)k_chain, hipFuncAttributeMaxDynamicSharedMemorySize, 100 * 1024);
  (void)hipFuncSetAttribute((const void*)k_post, hipFuncAttributeMaxDynamicSharedMemorySize, 84 * 1024);
  (void)hipFuncSetAttribute((const void*)k_ffn1, hipFuncAttributeMaxDynamicSharedMemorySize, 3 * 32 * 1024);
  (void)hipFuncSetAttribute((const void*)k_ffn2, hipFuncAttributeMaxDynamicSharedMemorySize, 3 * 36 * 1024);
  (void)hipMemsetAsync(ws + OFF_MEM, 0xFF, (size_t)65 * 10240 + (size_t)64 * 20480, stream);
  k_prep<<<PREP_BLOCKS, 256, 0, stream>>>(pa);
  k_wvo<<<80, 256, 0, stream>>>(pa.wv, ws);
  k_qkvu<<<512, 256, 3 * 44 * 1024, stream>>>(ws);
  k_chain<<<NH + NBK, 256, 100 * 1024, stream>>>(ws);
  k_post<<<64, 256, 84 * 1024, stream>>>(pa.x, ws);
  k_ffn1<<<128, 256, 3 * 32 * 1024, stream>>>(ws);
  k_ffn2<<<64, 256, 3 * 36 * 1024, stream>>>(ws, (u16*)d_out);
}
```

```cpp
#include <hip/hip_runtime.h>
#include <hip/hip_bf16.h>
#include <stdint.h>

constexpr int SEQ = 2048, DM = 640, NH = 32, DH = 192, NM = 8, SEG = 32, NSEG = 64, DF = 1536;
constexpr int KS_D = DM / 32;
constexpr int KS_F = DF / 32;
constexpr int NBK = 80;

constexpr size_t OFF_XNF  = 0;
constexpr size_t OFF_WQF  = OFF_XNF  + (size_t)128 * 20 * 1024;
constexpr size_t OFF_WKF  = OFF_WQF  + (size_t)384 * 20 * 1024;
constexpr size_t OFF_WVF  = OFF_WKF  + (size_t)384 * 20 * 1024;
constexpr size_t OFF_WOF  = OFF_WVF  + (size_t)384 * 20 * 1024;
constexpr size_t OFF_WGF  = OFF_WOF  + (size_t)32 * 40 * 6 * 1024;
constexpr size_t OFF_WUF  = OFF_WGF  + (size_t)96 * 20 * 1024;
constexpr size_t OFF_WDF  = OFF_WUF  + (size_t)96 * 20 * 1024;
constexpr size_t OFF_WVOF = OFF_WDF  + (size_t)40 * 48 * 1024;
constexpr size_t OFF_QX   = OFF_WVOF + (size_t)80 * 16 * 20 * 1024;
constexpr size_t OFF_KX   = OFF_QX   + (size_t)64 * 32 * 12288;
constexpr size_t OFF_UX   = OFF_KX   + (size_t)64 * 32 * 12288;
constexpr size_t OFF_MEM  = OFF_UX   + (size_t)64 * 80 * 16 * 1024;
constexpr size_t OFF_P    = OFF_MEM  + (size_t)65 * 10240;
constexpr size_t OFF_KM   = OFF_P    + (size_t)64 * 20480;
constexpr size_t OFF_UM   = OFF_KM   + (size_t)64 * 32 * 6144;
constexpr size_t OFF_PX   = OFF_UM   + (size_t)64 * 32 * 10240;
constexpr size_t OFF_HF   = OFF_PX   + (size_t)64 * 81920;
constexpr size_t OFF_ACTF = OFF_HF   + (size_t)128 * 20 * 1024;
constexpr size_t OFF_CNT  = OFF_ACTF + (size_t)128 * 48 * 1024;
constexpr int CNT_WVO = 0, CNT_M = 1, CNT_SEG = 17, CNT_PA = 81, CNT_PB = 145, CNT_F1 = 209, CNT_Q0 = 273, CNT_N = 275;
__host__ __device__ constexpr unsigned m_target(int m) { return m <= 1 ? 96u : 32u; }
constexpr size_t OFF_FLG  = OFF_CNT + (size_t)CNT_N * 256;
constexpr size_t CTRL_BYTES = (size_t)CNT_N * 256 + 64 * 512;
constexpr size_t WS_TOTAL = OFF_CNT + CTRL_BYTES + 256;
static_assert(WS_TOTAL < (size_t)256 * 1024 * 1024, "ws too big");

typedef short bf16x8 __attribute__((ext_vector_type(8)));
typedef float f32x4 __attribute__((ext_vector_type(4)));
typedef unsigned v4u __attribute__((ext_vector_type(4)));
typedef unsigned v2u __attribute__((ext_vector_type(2)));
typedef unsigned short u16;

__device__ __forceinline__ f32x4 mfma(v4u a, v4u b, f32x4 c) {
  return __builtin_amdgcn_mfma_f32_16x16x32_bf16(__builtin_bit_cast(bf16x8, a), __builtin_bit_cast(bf16x8, b), c, 0, 0, 0);
}
typedef __bf16 bf16x2_t __attribute__((ext_vector_type(2)));
typedef float f32x2 __attribute__((ext_vector_type(2)));
__device__ __forceinline__ unsigned cvtpk(float lo, float hi) {
  f32x2 v = {lo, hi};
  return __builtin_bit_cast(unsigned, __builtin_convertvector(v, bf16x2_t));
}
__device__ __forceinline__ float bf2f(unsigned short b) { return __builtin_bit_cast(float, (unsigned)b << 16); }
__device__ __forceinline__ float bflo(unsigned u) { return __builtin_bit_cast(float, u << 16); }
__device__ __forceinline__ float bfhi(unsigned u) { return __builtin_bit_cast(float, u & 0xffff0000u); }
__device__ __forceinline__ float rbf(float x) { return bflo(cvtpk(x, x)); }
__device__ __forceinline__ v4u pack8(f32x4 a, f32x4 b) {
  v4u r; r.x = cvtpk(a.x, a.y); r.y = cvtpk(a.z, a.w); r.z = cvtpk(b.x, b.y); r.w = cvtpk(b.z, b.w); return r;
}
__device__ __forceinline__ v2u pack4(f32x4 a) { v2u r; r.x = cvtpk(a.x, a.y); r.y = cvtpk(a.z, a.w); return r; }
constexpr unsigned SENT = 0xFFFFFFFFu;
__device__ __forceinline__ unsigned desent(unsigned x) { return x == SENT ? 0x7FC07FC0u : x; }
__device__ __forceinline__ bool isbad(v4u v) { return (v.x == SENT) | (v.y == SENT) | (v.z == SENT) | (v.w == SENT); }

#define RAW_BARRIER() do { asm volatile("s_waitcnt lgkmcnt(0)" ::: "memory"); __builtin_amdgcn_s_barrier(); } while (0)
#define WAIT_V(n) asm volatile("s_waitcnt vmcnt(" #n ")" ::: "memory")

__device__ __forceinline__ void glds16(const unsigned char* g, unsigned char* l) {
  __builtin_amdgcn_global_load_lds((const unsigned*)g, (unsigned*)l, 16, 0, 0);
}

typedef __amdgpu_buffer_rsrc_t rsrc_t;
__device__ __forceinline__ int lane_id() {
  int l; asm volatile("v_mbcnt_lo_u32_b32 %0, -1, 0\n\tv_mbcnt_hi_u32_b32 %0, -1, %0" : "=v"(l)); return l;
}
__device__ __forceinline__ void st16(rsrc_t rs, size_t off, v4u v) { __builtin_amdgcn_raw_buffer_store_b128(v, rs, (int)off, 0, 16); }
__device__ __forceinline__ void st8(rsrc_t rs, size_t off, v2u v) { __builtin_amdgcn_raw_buffer_store_b64(v, rs, (int)off, 0, 16); }
__device__ __forceinline__ unsigned* cnt_ptr(unsigned char* ws, int i) { return (unsigned*)(ws + OFF_CNT + (size_t)i * 256); }
constexpr unsigned SPIN_CAP = 1u << 22;
#define POLL_SLEEP 64
__device__ __forceinline__ void signal_done(unsigned char* ws, int w, int i) {
  asm volatile("s_waitcnt vmcnt(0)" ::: "memory");
  __syncthreads();
  if (w == 0 && lane_id() == 0) __hip_atomic_fetch_add(cnt_ptr(ws, i), 1u, __ATOMIC_RELAXED, __HIP_MEMORY_SCOPE_AGENT);
}
__device__ __forceinline__ void wait_count(unsigned char* ws, int w, int i, unsigned target) {
  if (w == 0 && lane_id() == 0) {
    unsigned* p = cnt_ptr(ws, i);
    for (unsigned spins = 0; spins < SPIN_CAP; ++spins) {
      if (__hip_atomic_load(p, __ATOMIC_RELAXED, __HIP_MEMORY_SCOPE_AGENT) >= target) break;
      __builtin_amdgcn_s_sleep(2);
    }
    __builtin_amdgcn_fence(__ATOMIC_ACQUIRE, "agent");
    asm volatile("s_waitcnt vmcnt(0)" ::: "memory");
  }
  __syncthreads();
}
__device__ __forceinline__ void wave_wait_count(unsigned char* ws, int i, unsigned target) {
  unsigned* p = cnt_ptr(ws, i);
  for (unsigned spins = 0; spins < SPIN_CAP; ++spins) {
    if (__builtin_amdgcn_readfirstlane(__hip_atomic_load(p, __ATOMIC_RELAXED, __HIP_MEMORY_SCOPE_AGENT)) >= target) break;
    __builtin_amdgcn_s_sleep(POLL_SLEEP);
  }
  __builtin_amdgcn_fence(__ATOMIC_ACQUIRE, "wavefront");
}
__device__ __forceinline__ void set_flag(unsigned char* ws, int seg, int i) {
  __hip_atomic_store((unsigned*)(ws + OFF_FLG + (size_t)seg * 512) + i, 1u, __ATOMIC_RELAXED, __HIP_MEMORY_SCOPE_AGENT);
}
__device__ __forceinline__ void wave_wait_flags(unsigned char* ws, int seg, int i0, int n, int lane) {
  unsigned* p = (unsigned*)(ws + OFF_FLG + (size_t)seg * 512) + i0 + (lane < n ? lane : 0);
  for (unsigned spins = 0; spins < SPIN_CAP; ++spins) {
    if (__all(__hip_atomic_load(p, __ATOMIC_RELAXED, __HIP_MEMORY_SCOPE_AGENT) != 0u)) break;
    __builtin_amdgcn_s_sleep(POLL_SLEEP);
  }
  __builtin_amdgcn_fence(__ATOMIC_ACQUIRE, "wavefront");
}

__device__ __forceinline__ void swap16(float v, float& a, float& b) {
  a = v; b = v;
  asm volatile("s_nop 1\n\tv_permlane16_swap_b32 %0, %1\n\ts_nop 1" : "+v"(a), "+v"(b));
}
__device__ __forceinline__ void swap32(float v, float& a, float& b) {
  a = v; b = v;
  asm volatile("s_nop 1\n\tv_permlane32_swap_b32 %0, %1\n\ts_nop 1" : "+v"(a), "+v"(b));
}
__device__ __forceinline__ float xmax16(float v) { float a, b; swap16(v, a, b); return fmaxf(a, b); }
__device__ __forceinline__ float xmax32(float v) { float a, b; swap32(v, a, b); return fmaxf(a, b); }
__device__ __forceinline__ float xadd16(float v) { float a, b; swap16(v, a, b); return a + b; }
__device__ __forceinline__ float xadd32(float v) { float a, b; swap32(v, a, b); return a + b; }
__device__ __forceinline__ v4u ld_sc1(__amdgpu_buffer_rsrc_t rs, int off) { return __builtin_amdgcn_raw_buffer_load_b128(rs, off, 0, 16); }
__device__ __forceinline__ v2u ld8_sc1(__amdgpu_buffer_rsrc_t rs, int off) { return __builtin_amdgcn_raw_buffer_load_b64(rs, off, 0, 16); }
__device__ __forceinline__ void swapu16(unsigned& a, unsigned& b) { asm volatile("s_nop 1\n\tv_permlane16_swap_b32 %0, %1\n\ts_nop 1" : "+v"(a), "+v"(b)); }

struct PrepArgs {
  const u16* x; const u16* mem0; const u16* wq; const u16* wk; const u16* wv; const u16* wo;
  const u16* wg; const u16* wu; const u16* wd; unsigned char* ws;
};

__device__ __forceinline__ void relayout_tile(const u16* src, int ld, int k0, int n0, unsigned char* dst, int ks_per_nt,
                                              int ks_idx, int nt0, bool perm, u16* tile) {
  const int t = threadIdx.x;
#pragma unroll
  for (int i = 0; i < 2; ++i) {
    const int id = t + 256 * i, row = id >> 4, c8 = id & 15;
    const v4u v = *(const v4u*)(src + (size_t)(k0 + row) * ld + n0 + c8 * 8);
    *(v4u*)(tile + row * 136 + c8 * 8) = v;
  }
  __syncthreads();
#pragma unroll
  for (int i = 0; i < 2; ++i) {
    const int id = t + 256 * i, f = id >> 6, lane = id & 63, g = lane >> 4, n = lane & 15;
    unsigned w[4];
#pragma unroll
    for (int jj = 0; jj < 4; ++jj) {
      const int j0 = 2 * jj, j1 = 2 * jj + 1;
      const int ka = perm ? (16 * (j0 >> 2) + 4 * g + (j0 & 3)) : (8 * g + j0);
      const int kb = perm ? (16 * (j1 >> 2) + 4 * g + (j1 & 3)) : (8 * g + j1);
      w[jj] = (unsigned)tile[ka * 136 + 16 * f + n] | ((unsigned)tile[kb * 136 + 16 * f + n] << 16);
    }
    v4u o = {w[0], w[1], w[2], w[3]};
    *(v4u*)(dst + ((size_t)(nt0 + f) * ks_per_nt + ks_idx) * 1024 + lane * 16) = o;
  }
}

constexpr int PREP_LN = 128, PREP_WQKV = 960, PREP_WO = 960, PREP_WGU = 240, PREP_WD = 240;
constexpr int PREP_BLOCKS = PREP_LN + 3 * PREP_WQKV + PREP_WO + 2 * PREP_WGU + PREP_WD + 1;

__global__ void __launch_bounds__(256) k_prep(PrepArgs a) {
  __shared__ __attribute__((aligned(16))) u16 tile[32 * 136];
  int b = blockIdx.x;
  const int t = threadIdx.x;
  if (b < PREP_LN) {
    const int lane = t & 63, w = t >> 6, r = lane >> 4, c = lane & 15;
    const int row = b * 16 + w * 4 + r;
    v4u v[5];
    float s = 0.f;
#pragma unroll
    for (int i = 0; i < 5; ++i) {
      v[i] = *(const v4u*)(a.x + (size_t)row * DM + (c + 16 * i) * 8);
      s += bflo(v[i].x) + bfhi(v[i].x) + bflo(v[i].y) + bfhi(v[i].y) + bflo(v[i].z) + bfhi(v[i].z) + bflo(v[i].w) + bfhi(v[i].w);
    }
#pragma unroll
    for (int o = 1; o < 16; o <<= 1) s += __shfl_xor(s, o);
    const float mean = s * (1.f / DM);
    float q = 0.f;
#pragma unroll
    for (int i = 0; i < 5; ++i) {
      const unsigned u[4] = {v[i].x, v[i].y, v[i].z, v[i].w};
#pragma unroll
      for (int j = 0; j < 4; ++j) { float d0 = bflo(u[j]) - mean, d1 = bfhi(u[j]) - mean; q += d0 * d0 + d1 * d1; }
    }
#pragma unroll
    for (int o = 1; o < 16; o <<= 1) q += __shfl_xor(q, o);
    const float rs = rsqrtf(q * (1.f / DM) + 1e-5f);
#pragma unroll
    for (int i = 0; i < 5; ++i) {
      const unsigned u[4] = {v[i].x, v[i].y, v[i].z, v[i].w};
      v4u o;
      o.x = cvtpk((bflo(u[0]) - mean) * rs, (bfhi(u[0]) - mean) * rs);
      o.y = cvtpk((bflo(u[1]) - mean) * rs, (bfhi(u[1]) - mean) * rs);
      o.z = cvtpk((bflo(u[2]) - mean) * rs, (bfhi(u[2]) - mean) * rs);
      o.w = cvtpk((bflo(u[3]) - mean) * rs, (bfhi(u[3]) - mean) * rs);
      const int ch = c + 16 * i;
      *(v4u*)(a.ws + OFF_XNF + ((size_t)(row >> 4) * KS_D + (ch >> 2)) * 1024 + ((ch & 3) * 16 + (row & 15)) * 16) = o;
    }
    return;
  }
  b -= PREP_LN;
  if (b < 3 * PREP_WQKV) {
    const int which = b / PREP_WQKV, r = b % PREP_WQKV, ks = r % 20, nc = r / 20;
    const u16* src = which == 0 ? a.wq : (which == 1 ? a.wk : a.wv);
    unsigned char* dst = a.ws + (which == 0 ? OFF_WQF : (which == 1 ? OFF_WKF : OFF_WVF));
    relayout_tile(src, NH * DH, ks * 32, nc * 128, dst, KS_D, ks, nc * 8, false, tile);
    return;
  }
  b -= 3 * PREP_WQKV;
  if (b < PREP_WO) {
    const int kk = b % 192, nc = b / 192, h = kk / 6, pr = kk % 6;
    relayout_tile(a.wo, DM, kk * 32, nc * 128, a.ws + OFF_WOF + (size_t)h * 40 * 6 * 1024, 6, pr, nc * 8, true, tile);
    return;
  }
  b -= PREP_WO;
  if (b < 2 * PREP_WGU) {
    const int which = b / PREP_WGU, r = b % PREP_WGU, ks = r % 20, nc = r / 20;
    relayout_tile(which ? a.wu : a.wg, DF, ks * 32, nc * 128, a.ws + (which ? OFF_WUF : OFF_WGF), KS_D, ks, nc * 8, true, tile);
    return;
  }
  b -= 2 * PREP_WGU;
  if (b < PREP_WD) {
    const int ks = b % 48, nc = b / 48;
    relayout_tile(a.wd, DM, ks * 32, nc * 128, a.ws + OFF_WDF, KS_F, ks, nc * 8, true, tile);
    return;
  }
  for (int id = t; id < 640; id += 256) {
    const int nb = id >> 3, tok = id & 7;
    v4u v = *(const v4u*)(a.mem0 + tok * DM + nb * 8);
    v.x = desent(v.x); v.y = desent(v.y); v.z = desent(v.z); v.w = desent(v.w);
    *(v4u*)(a.ws + OFF_MEM + id * 16) = v;
  }
}

#define MFMA2_A(c0, c1, a, b0, b1) asm volatile("v_mfma_f32_16x16x32_bf16 %0, %2, %3, %0\n\tv_mfma_f32_16x16x32_bf16 %1, %2, %4, %1" \
      : "+a"(c0), "+a"(c1) : "v"(a), "v"(b0), "v"(b1))
#define MFMA2_V(c0, c1, a, b0, b1) asm volatile("v_mfma_f32_16x16x32_bf16 %0, %2, %3, %0\n\tv_mfma_f32_16x16x32_bf16 %1, %2, %4, %1" \
      : "+v"(c0), "+v"(c1) : "v"(a), "v"(b0), "v"(b1))
#define MFMA2B_V(c0, c1, a0, a1, b) asm volatile("v_mfma_f32_16x16x32_bf16 %0, %2, %4, %0\n\tv_mfma_f32_16x16x32_bf16 %1, %3, %4, %1" \
      : "+v"(c0), "+v"(c1) : "v"(a0), "v"(a1), "v"(b))
template <int N> __device__ __forceinline__ void wait_v() { asm volatile("s_waitcnt vmcnt(%0)" ::"n"(N) : "memory"); }

template <int NA>
__device__ __forceinline__ void proj_gemm(unsigned char* ws, unsigned char* smem, int w, int lane, int m, int h, unsigned wsel, f32x4 (&acc)[NA][2]) {
  constexpr int FPK = NA + 8, LPW = FPK / 4, BUFB = FPK * 1024, GI = NA / LPW;
#pragma unroll
  for (int a = 0; a < NA; ++a) { acc[a][0] = f32x4{0, 0, 0, 0}; acc[a][1] = f32x4{0, 0, 0, 0}; }
  unsigned soff[LPW];
#pragma unroll
  for (int i = 0; i < LPW; ++i) {
    const int ff = w * LPW + i;
    const unsigned base = NA == 12 ? wsel : (ff < 12 ? (unsigned)OFF_WQF : (ff < 24 ? (unsigned)OFF_WKF : (unsigned)OFF_WVF));
    const int et = ff % 12;
    const unsigned o = ff < NA ? base + (unsigned)(h * 12 + et) * (KS_D * 1024u) : (unsigned)OFF_XNF + (unsigned)(m * 8 + ff - NA) * (KS_D * 1024u);
    soff[i] = __builtin_amdgcn_readfirstlane(o);
  }
  const unsigned char* gl = ws + lane * 16;
#pragma unroll
  for (int st = 0; st < 3; ++st)
#pragma unroll
    for (int i = 0; i < LPW; ++i) glds16(gl + (soff[i] + (unsigned)st * 1024u), smem + st * BUFB + (w * LPW + i) * 1024);
  wait_v<2 * LPW>();
  RAW_BARRIER();
  v4u af[NA], b0, b1;
  {
    const unsigned char* buf = smem + lane * 16;
    b0 = *(const v4u*)(buf + (NA + 2 * w) * 1024); b1 = *(const v4u*)(buf + (NA + 2 * w + 1) * 1024);
#pragma unroll
    for (int a = 0; a < NA; ++a) af[a] = *(const v4u*)(buf + a * 1024);
  }
  int bn = 1;
  for (int i = 0; i < KS_D - 1; ++i) {
    if (i + 2 < KS_D) { wait_v<LPW>(); } else { wait_v<0>(); }
    RAW_BARRIER();
    const int bf = bn == 0 ? 2 : bn - 1;
    const bool pre = i + 3 < KS_D;
    const unsigned char* buf = smem + bn * BUFB + lane * 16;
    const v4u nb0 = *(const v4u*)(buf + (NA + 2 * w) * 1024), nb1 = *(const v4u*)(buf + (NA + 2 * w + 1) * 1024);
    asm volatile("s_nop 1");
#pragma unroll
    for (int a = 0; a < NA; ++a) {
      if (a < 32) MFMA2_A(acc[a][0], acc[a][1], af[a], b0, b1); else MFMA2_V(acc[a][0], acc[a][1], af[a], b0, b1);
      af[a] = *(const v4u*)(buf + a * 1024);
      if (a % GI == 1 && a / GI < LPW && pre) glds16(gl + (soff[a / GI] + (unsigned)(i + 3) * 1024u), smem + bf * BUFB + (w * LPW + a / GI) * 1024);
    }
    b0 = nb0; b1 = nb1;
    bn = bn == 2 ? 0 : bn + 1;
  }
  asm volatile("s_nop 1");
#pragma unroll
  for (int a = 0; a < NA; ++a) {
    if (a < 32) MFMA2_A(acc[a][0], acc[a][1], af[a], b0, b1); else MFMA2_V(acc[a][0], acc[a][1], af[a], b0, b1);
  }
  asm volatile("s_nop 7\n\ts_nop 7" ::: "memory");
#pragma unroll
  for (int a = 0; a < NA; ++a) { if (a < 32) asm volatile("" : "+a"(acc[a][0]), "+a"(acc[a][1])); else asm volatile("" : "+v"(acc[a][0]), "+v"(acc[a][1])); }
}

__device__ __forceinline__ void u_issue(unsigned char* ws, unsigned char* smem, int w, int lane, int h, int st, int buf) {
  const unsigned char* wo = ws + OFF_WOF + (size_t)h * 40 * 6 * 1024 + lane * 16;
#pragma unroll
  for (int i = 0; i < 6; ++i) glds16(wo + (size_t)(st * 24 + w * 6 + i) * 1024, smem + buf * 24576 + (w * 6 + i) * 1024);
}
template <int EPI, int PRE>
__device__ __forceinline__ void u_stage(unsigned char* ws, rsrc_t rs, unsigned char* smem, int w, int lane, int h, const v4u (&vf)[2][6],
                                        int st0, int nst, size_t obase) {
  wait_v<12 + PRE>();
  RAW_BARRIER();
  v4u bf[24];
#pragma unroll
  for (int i = 0; i < 24; ++i) bf[i] = *(const v4u*)(smem + i * 1024 + lane * 16);
  int bn = 1;
  for (int i0 = 0; i0 < nst; ++i0) {
    const bool more = i0 + 1 < nst;
    if (more) {
      if (i0 == 0) { wait_v<6 + PRE>(); } else if (i0 == 1) { wait_v<10 + PRE>(); } else if (i0 + 2 < nst) { wait_v<14>(); } else { wait_v<8>(); }
      RAW_BARRIER();
      const int bfree = bn == 0 ? 2 : bn - 1;
      if (i0 + 3 < nst) u_issue(ws, smem, w, lane, h, st0 + i0 + 3, bfree);
    }
    const unsigned char* buf = smem + bn * 24576 + lane * 16;
    f32x4 d0[4], d1[4];
#pragma unroll
    for (int i = 0; i < 4; ++i) { d0[i] = f32x4{0, 0, 0, 0}; d1[i] = f32x4{0, 0, 0, 0}; }
    asm volatile("s_nop 1" : "+v"(d0[0]), "+v"(d1[0]), "+v"(d0[1]), "+v"(d1[1]), "+v"(d0[2]), "+v"(d1[2]), "+v"(d0[3]), "+v"(d1[3]));
#pragma unroll
    for (int p = 0; p < 6; ++p)
#pragma unroll
      for (int i = 0; i < 4; ++i) {
        MFMA2B_V(d0[i], d1[i], vf[0][p], vf[1][p], bf[i * 6 + p]);
        if (more) bf[i * 6 + p] = *(const v4u*)(buf + (i * 6 + p) * 1024);
      }
    asm volatile("s_nop 7\n\ts_nop 1" : "+v"(d0[0]), "+v"(d1[0]), "+v"(d0[1]), "+v"(d1[1]), "+v"(d0[2]), "+v"(d1[2]), "+v"(d0[3]), "+v"(d1[3]));
    const int nt0 = (st0 + i0) * 4;
#pragma unroll
    for (int i = 0; i < 4; ++i) {
      if (EPI == 0) {
        st16(rs, obase + (size_t)(nt0 + i) * 2 * 16 * 1024, pack8(d0[i], d1[i]));
      } else {
        const v2u a = pack4(d0[i]), b = pack4(d1[i]);
        unsigned ax = a.x, ay = a.y, bx = b.x, by = b.y;
        swapu16(ax, bx); swapu16(ay, by);
        st16(rs, obase + (size_t)(nt0 + i) * 2 * 16 * 20 * 1024, v4u{ax, ay, bx, by});
      }
    }
    bn = bn == 2 ? 0 : bn + 1;
  }
}

template <int NB4>
__device__ __forceinline__ void store_qk(rsrc_t rs, size_t base, int lane, const f32x4 (&acc)[NB4][2], int a0) {
#pragma unroll
  for (int T = 0; T < 2; ++T)
#pragma unroll
    for (int gr = 0; gr < 4; ++gr) {
      const size_t p = base + (T * 4 + gr) * 1536;
      st16(rs, p + lane * 16, pack8(acc[a0 + 3 * gr][T], acc[a0 + 3 * gr + 1][T]));
      st8(rs, p + 1024 + lane * 8, pack4(acc[a0 + 3 * gr + 2][T]));
    }
}
__device__ __forceinline__ size_t ux_base(int seg, int h, int lane) {
  const int g = lane >> 4, l16 = lane & 15;
  return OFF_UX + ((size_t)(seg * 80 + (l16 >> 3)) * 16 + (h >> 1)) * 1024 + (g * 16 + (h & 1) * 8 + (l16 & 7)) * 16;
}

__device__ __forceinline__ void item_qkvu(unsigned char* ws, rsrc_t rs, unsigned char* smem, int w, int m, int h) {
  const int lane = lane_id();
  f32x4 acc[36][2];
  proj_gemm<36>(ws, smem, w, lane, m, h, 0u, acc);
  RAW_BARRIER();
  u_issue(ws, smem, w, lane, h, 0, 0); u_issue(ws, smem, w, lane, h, 1, 1); u_issue(ws, smem, w, lane, h, 2, 2);
  const int seg = m * 4 + w;
  v4u vf[2][6];
#pragma unroll
  for (int T = 0; T < 2; ++T)
#pragma unroll
    for (int p = 0; p < 6; ++p) vf[T][p] = pack8(acc[24 + 2 * p][T], acc[24 + 2 * p + 1][T]);
  store_qk<36>(rs, OFF_QX + (size_t)(seg * 32 + h) * 12288, lane, acc, 0);
  store_qk<36>(rs, OFF_KX + (size_t)(seg * 32 + h) * 12288, lane, acc, 12);
  u_stage<0, 32>(ws, rs, smem, w, lane, h, vf, 0, 10, ux_base(seg, h, lane));
}
__device__ __forceinline__ void item_piece(unsigned char* ws, rsrc_t rs, unsigned char* smem, int w, int m, int h, int kind) {
  const int lane = lane_id();
  f32x4 acc[12][2];
  proj_gemm<12>(ws, smem, w, lane, m, h, kind == 0 ? (unsigned)OFF_WQF : (kind == 1 ? (unsigned)OFF_WKF : (unsigned)OFF_WVF), acc);
  const int seg = m * 4 + w;
  if (kind < 2) {
    store_qk<12>(rs, (kind ? OFF_KX : OFF_QX) + (size_t)(seg * 32 + h) * 12288, lane, acc, 0);
  } else {
    const int st0 = (kind - 2) * 5;
    RAW_BARRIER();
    u_issue(ws, smem, w, lane, h, st0, 0); u_issue(ws, smem, w, lane, h, st0 + 1, 1); u_issue(ws, smem, w, lane, h, st0 + 2, 2);
    v4u vf[2][6];
#pragma unroll
    for (int T = 0; T < 2; ++T)
#pragma unroll
      for (int p = 0; p < 6; ++p) vf[T][p] = pack8(acc[2 * p][T], acc[2 * p + 1][T]);
    u_stage<0, 0>(ws, rs, smem, w, lane, h, vf, st0, 5, ux_base(seg, h, lane));
  }
}
__device__ __forceinline__ void item_wvo(const u16* wv, unsigned char* ws, rsrc_t rs, unsigned char* smem, int w, int h, int part) {
  const int lane = lane_id(), g = lane >> 4, l16 = lane & 15;
  const int ks = part * 4 + w;
  v4u vf[2][6];
#pragma unroll
  for (int T = 0; T < 2; ++T)
#pragma unroll
    for (int p = 0; p < 6; ++p) {
      const u16* src = wv + (size_t)(ks * 32 + T * 16 + l16) * (NH * DH) + h * DH + 32 * p + 4 * g;
      const v2u lo = *(const v2u*)src, hi = *(const v2u*)(src + 16);
      vf[T][p] = v4u{lo.x, lo.y, hi.x, hi.y};
    }
  wait_v<0>();
  u_issue(ws, smem, w, lane, h, 0, 0); u_issue(ws, smem, w, lane, h, 1, 1); u_issue(ws, smem, w, lane, h, 2, 2);
  const int gp = ((g & 1) << 1) | (g >> 1);
  const size_t ob = OFF_WVOF + ((size_t)((l16 >> 3) * 16 + (h >> 1)) * 20 + ks) * 1024 + (gp * 16 + (h & 1) * 8 + (l16 & 7)) * 16;
  u_stage<1, 0>(ws, rs, smem, w, lane, h, vf, 0, 10, ob);
}

#define FS_DECL do {} while (0)
#define FS(i) do {} while (0)
#define FS_FLUSH() do {} while (0)
#define STAMP(i) do {} while (0)
#define TL(slot) do {} while (0)
__device__ __forceinline__ unsigned long long now() { return __builtin_amdgcn_s_memtime(); }
__device__ __forceinline__ void wait_until(unsigned long long t) { while ((long long)(now() - t) < 0) __builtin_amdgcn_s_sleep(1); }
#define DQ0 4000
#define DCM0 2200
#define DCP0 4000
#define T_DN 24
#define T_UP 160

__device__ __forceinline__ unsigned fetch_mem(__amdgpu_buffer_rsrc_t rs, int s, unsigned char* lmem) {
  const int tid = threadIdx.x;
  const int base = (int)OFF_MEM + s * 10240;
  const int o0 = base + tid * 16, o1 = base + (tid + 256) * 16, o2 = base + ((tid & 127) + 512) * 16;
  v4u m0, m1, m2;
  unsigned spins = 0;
  for (; spins < SPIN_CAP; ++spins) {
    asm volatile("" ::: "memory");
    m0 = ld_sc1(rs, o0); m1 = ld_sc1(rs, o1); m2 = ld_sc1(rs, o2);
    const bool ok = !(isbad(m0) | isbad(m1) | isbad(m2));
    if (__all(ok)) break;
  }
  *(v4u*)(lmem + tid * 16) = m0;
  *(v4u*)(lmem + (tid + 256) * 16) = m1;
  *(v4u*)(lmem + ((tid & 127) + 512) * 16) = m2;
  return spins;
}

struct ChainT { int dq0, dcm0, dcp0, dn, up, pad; };
__device__ __forceinline__ void chain_role(const u16* wvin, unsigned char* ws, rsrc_t rs, unsigned char* smem, ChainT ct) {
  const int tid = threadIdx.x, lane = tid & 63, w = __builtin_amdgcn_readfirstlane(tid >> 6), g = lane >> 4, l16 = lane & 15;
  unsigned char* lmem = smem;
  float* red = (float*)(smem + 10240);
  unsigned char* lw = smem + 10240 + 12288;
  const int bfo = g * 128 + (l16 & 7) * 16;
  if (blockIdx.x < NH) {
    const int h = blockIdx.x;
    item_piece(ws, rs, smem, w, 0, h, 1);
    signal_done(ws, w, CNT_M);
    v4u wa[60], wv[40];
    auto wsrc = [&](int ks, int tl) -> const unsigned char* {
      return ws + (tl < 3 ? OFF_WQF : OFF_WKF) + ((size_t)(h * 12 + 3 * w + (tl % 3)) * KS_D + ks) * 1024 + lane * 16;
    };
#pragma unroll
    for (int ks = 0; ks < KS_D; ++ks) {
#pragma unroll
      for (int t = 0; t < 3; ++t) wa[ks * 3 + t] = *(const v4u*)wsrc(ks, t);
#pragma unroll
      for (int t = 0; t < 2; ++t) wv[ks * 2 + t] = *(const v4u*)wsrc(ks, 3 + t);
      *(v4u*)(lw + (w * KS_D + ks) * 1024 + lane * 16) = *(const v4u*)wsrc(ks, 5);
    }
    const unsigned char* lwl = lw + w * KS_D * 1024 + lane * 16;
    const float scale = 0.07216878364870322f * 1.4426950408889634f;
    unsigned long long tref = 0; int dq = ct.dq0;
    for (int s = 0; s < NSEG; ++s) {
      if ((s & 3) == 0) wave_wait_count(ws, CNT_M + (s >> 2), m_target(s >> 2));
      const int kx = (int)OFF_KX + (s * 32 + h) * 12288 + w * 1536;
      const v4u kp0 = ld_sc1(rs, kx + lane * 16), kp1 = ld_sc1(rs, kx + 4 * 1536 + lane * 16);
      const v2u ks0 = ld8_sc1(rs, kx + 1024 + lane * 8), ks1 = ld8_sc1(rs, kx + 4 * 1536 + 1024 + lane * 8);
      v4u wl[3];
      wl[0] = *(const v4u*)(lwl); wl[1] = *(const v4u*)(lwl + 1024);
      FS_DECL;
      if (s > 0) wait_until(tref + dq);
      FS(0);
      { const unsigned sp = fetch_mem(rs, s, lmem); if (s > 0) dq += sp ? ct.up : -ct.dn;
      }
      FS(1);
      __syncthreads();
      FS(2);
      TL(s);
      if (s > 0 && tid == 64) set_flag(ws, s - 1, h);
      f32x4 acc[6];
#pragma unroll
      for (int t = 0; t < 6; ++t) acc[t] = f32x4{0, 0, 0, 0};
      v4u bq[3];
      bq[0] = *(const v4u*)(lmem + bfo); bq[1] = *(const v4u*)(lmem + 512 + bfo);
#pragma unroll
      for (int ks = 0; ks < KS_D; ++ks) {
        if (ks + 2 < KS_D) {
          bq[(ks + 2) % 3] = *(const v4u*)(lmem + (ks + 2) * 512 + bfo);
          wl[(ks + 2) % 3] = *(const v4u*)(lwl + (ks + 2) * 1024);
        }
        if (ks == 0) asm volatile("s_nop 1" : "+v"(acc[0]), "+v"(acc[1]), "+v"(acc[2]), "+v"(acc[3]), "+v"(acc[4]), "+v"(acc[5]));
        asm volatile(
            "v_mfma_f32_16x16x32_bf16 %0, %6, %12, %0\n\t"
            "v_mfma_f32_16x16x32_bf16 %1, %7, %12, %1\n\t"
            "v_mfma_f32_16x16x32_bf16 %2, %8, %12, %2\n\t"
            "v_mfma_f32_16x16x32_bf16 %3, %9, %12, %3\n\t"
            "v_mfma_f32_16x16x32_bf16 %4, %10, %12, %4\n\t"
            "v_mfma_f32_16x16x32_bf16 %5, %11, %12, %5"
            : "+v"(acc[0]), "+v"(acc[1]), "+v"(acc[2]), "+v"(acc[3]), "+v"(acc[4]), "+v"(acc[5])
            : "a"(wa[ks * 3]), "a"(wa[ks * 3 + 1]), "a"(wa[ks * 3 + 2]), "v"(wv[ks * 2]), "v"(wv[ks * 2 + 1]), "v"(wl[ks % 3]), "v"(bq[ks % 3]));
      }
      asm volatile("s_nop 7\n\ts_nop 1" : "+v"(acc[0]), "+v"(acc[1]), "+v"(acc[2]), "+v"(acc[3]), "+v"(acc[4]), "+v"(acc[5]));
      FS(3);
      const v4u qp = pack8(acc[0], acc[1]);
      const v2u qs2 = pack4(acc[2]);
      const v4u qs = {qs2.x, qs2.y, 0u, 0u};
      const v4u kmp = pack8(acc[3], acc[4]);
      const v2u kms2 = pack4(acc[5]);
      const v4u kms = {kms2.x, kms2.y, 0u, 0u};
      f32x4 z = {0, 0, 0, 0};
      f32x4 s0 = mfma(kmp, qp, z); s0 = mfma(kms, qs, s0);
      f32x4 s1 = mfma(kp0, qp, z); s1 = mfma(v4u{ks0.x, ks0.y, 0u, 0u}, qs, s1);
      f32x4 s2 = mfma(kp1, qp, z); s2 = mfma(v4u{ks1.x, ks1.y, 0u, 0u}, qs, s2);
      if (w != 0) {
        *(f32x4*)(red + ((w * 3 + 0) * 64 + lane) * 4) = s0;
        *(f32x4*)(red + ((w * 3 + 1) * 64 + lane) * 4) = s1;
        *(f32x4*)(red + ((w * 3 + 2) * 64 + lane) * 4) = s2;
      }
      FS(4);
      __syncthreads();
      tref = now();
      FS(5);
      if (w == 0) {
        f32x4 r_[9];
#pragma unroll
        for (int i = 0; i < 9; ++i) r_[i] = *(const f32x4*)(red + ((3 + i) * 64 + lane) * 4);
        __builtin_amdgcn_sched_barrier(0);
        f32x4 t0 = (s0 + r_[0]) + (r_[3] + r_[6]), t1 = (s1 + r_[1]) + (r_[4] + r_[7]), t2 = (s2 + r_[2]) + (r_[5] + r_[8]);
        const bool mv = g < 2;
        if (!mv) t0 = t1;
        float mx = fmaxf(fmaxf(fmaxf(t1.x, t1.y), fmaxf(t1.z, t1.w)), fmaxf(fmaxf(t2.x, t2.y), fmaxf(t2.z, t2.w)));
        mx = fmaxf(mx, fmaxf(fmaxf(t0.x, t0.y), fmaxf(t0.z, t0.w)));
        mx = xmax32(xmax16(mx));
        const float mxs = mx * scale;
        f32x4 e0, e1, e2;
        e0.x = __builtin_amdgcn_exp2f(t0.x * scale - mxs); e0.y = __builtin_amdgcn_exp2f(t0.y * scale - mxs);
        e0.z = __builtin_amdgcn_exp2f(t0.z * scale - mxs); e0.w = __builtin_amdgcn_exp2f(t0.w * scale - mxs);
        e1.x = __builtin_amdgcn_exp2f(t1.x * scale - mxs); e1.y = __builtin_amdgcn_exp2f(t1.y * scale - mxs);
        e1.z = __builtin_amdgcn_exp2f(t1.z * scale - mxs); e1.w = __builtin_amdgcn_exp2f(t1.w * scale - mxs);
        e2.x = __builtin_amdgcn_exp2f(t2.x * scale - mxs); e2.y = __builtin_amdgcn_exp2f(t2.y * scale - mxs);
        e2.z = __builtin_amdgcn_exp2f(t2.z * scale - mxs); e2.w = __builtin_amdgcn_exp2f(t2.w * scale - mxs);
        if (!mv) e0 = z;
        float sum = ((e0.x + e0.y) + (e0.z + e0.w)) + ((e1.x + e1.y) + (e1.z + e1.w)) + ((e2.x + e2.y) + (e2.z + e2.w));
        sum = xadd32(xadd16(sum));
        const float inv = __builtin_amdgcn_rcpf(sum);
        e0 *= inv; e1 *= inv; e2 *= inv;
        v4u px = pack8(e1, e2);
        v2u pm = pack4(e0);
        px.x &= 0x7FFF7FFFu; px.y &= 0x7FFF7FFFu; px.z &= 0x7FFF7FFFu; px.w &= 0x7FFF7FFFu;
        pm.x &= 0x7FFF7FFFu; pm.y &= 0x7FFF7FFFu;
        const int pb = (int)OFF_P + s * 20480 + (h >> 3) * 5120;
        if (l16 < 8) {
          __builtin_amdgcn_raw_buffer_store_b128(px, rs, pb + ((h >> 1) & 3) * 1024 + (g * 16 + (h & 1) * 8 + l16) * 16, 0, 16);
          if (mv) __builtin_amdgcn_raw_buffer_store_b64(pm, rs, pb + 4096 + (((h >> 1) & 3) * 16 + (h & 1) * 8 + l16) * 16 + g * 8, 0, 16);
        }
      }
      FS(6);
      FS_FLUSH();
      const size_t km = OFF_KM + (size_t)(s * 32 + h) * 6144 + w * 1536;
      st16(rs, km + lane * 16, kmp);
      st8(rs, km + 1024 + lane * 8, kms2);
    }
    asm volatile("s_waitcnt vmcnt(0)" ::: "memory");
    __syncthreads();
    if (tid == 0) set_flag(ws, NSEG - 1, h);
    TL(64);
  } else if (blockIdx.x < NH + NBK) {
    const int nb = blockIdx.x - NH;
    item_wvo(wvin, ws, rs, smem, w, nb / 5, nb % 5);
    signal_done(ws, w, CNT_WVO);
    TL(1);
    wait_count(ws, w, CNT_WVO, 160);
    TL(2);
    v4u wa[60], wv[20];
#pragma unroll
    for (int ks = 0; ks < KS_D; ++ks) {
#pragma unroll
      for (int t = 0; t < 3; ++t)
        wa[ks * 3 + t] = *(const v4u*)(ws + OFF_WVOF + ((size_t)(nb * 16 + 4 * w + t) * 20 + ks) * 1024 + lane * 16);
      wv[ks] = *(const v4u*)(ws + OFF_WVOF + ((size_t)(nb * 16 + 4 * w + 3) * 20 + ks) * 1024 + lane * 16);
    }
    asm volatile("s_waitcnt vmcnt(0)" ::: "memory");
    TL(3);
    unsigned char* lu = lw + w * 1024;
    unsigned long long tref = 0, tmem = 0; int dcm = ct.dcm0, dcp = ct.dcp0;
    for (int s = 0; s < NSEG; ++s) {
      if ((s & 3) == 0) wave_wait_count(ws, CNT_M + (s >> 2), m_target(s >> 2));
      v4u ux[4];
#pragma unroll
      for (int t = 0; t < 4; ++t) ux[t] = ld_sc1(rs, (int)OFF_UX + ((s * 80 + nb) * 16 + 4 * w + t) * 1024 + lane * 16);
      FS_DECL;
      if (s > 0) wait_until(tref + dcm);
      FS(0);
      { const unsigned sp = fetch_mem(rs, s, lmem); if (s > 0) dcm += sp ? ct.up : -ct.dn; }
      FS(1);
      __syncthreads();
      tmem = now();
      FS(2);
      f32x4 acc[4];
#pragma unroll
      for (int t = 0; t < 4; ++t) acc[t] = f32x4{0, 0, 0, 0};
      v4u aq[3];
      aq[0] = *(const v4u*)(lmem + bfo); aq[1] = *(const v4u*)(lmem + 512 + bfo);
#pragma unroll
      for (int ks = 0; ks < KS_D; ++ks) {
        if (ks + 2 < KS_D) aq[(ks + 2) % 3] = *(const v4u*)(lmem + (ks + 2) * 512 + bfo);
        if (ks == 0) asm volatile("s_nop 1" : "+v"(acc[0]), "+v"(acc[1]), "+v"(acc[2]), "+v"(acc[3]));
        asm volatile(
            "v_mfma_f32_16x16x32_bf16 %0, %8, %4, %0\n\t"
            "v_mfma_f32_16x16x32_bf16 %1, %8, %5, %1\n\t"
            "v_mfma_f32_16x16x32_bf16 %2, %8, %6, %2\n\t"
            "v_mfma_f32_16x16x32_bf16 %3, %8, %7, %3"
            : "+v"(acc[0]), "+v"(acc[1]), "+v"(acc[2]), "+v"(acc[3])
            : "a"(wa[ks * 3]), "a"(wa[ks * 3 + 1]), "a"(wa[ks * 3 + 2]), "v"(wv[ks]), "v"(aq[ks % 3]));
      }
      asm volatile("s_nop 7\n\ts_nop 1" : "+v"(acc[0]), "+v"(acc[1]), "+v"(acc[2]), "+v"(acc[3]));
      if (g < 2) {
#pragma unroll
        for (int t = 0; t < 4; ++t) *(v2u*)(lu + (t * 16 + l16) * 16 + g * 8) = pack4(acc[t]);
      }
      asm volatile("s_waitcnt lgkmcnt(0)" ::: "memory");
      const v4u um = *(const v4u*)(lu + (g * 16 + l16) * 16);
      st16(rs, OFF_UM + ((size_t)(s * 32 + 8 * w + 2 * g + (l16 >> 3)) * 640 + 8 * nb + (l16 & 7)) * 16, um);
      const int pb = (int)OFF_P + s * 20480 + w * 5120 + lane * 16;
      v4u p0, p1, p2, p3, p4;
      FS(3);
      wait_until(tmem + dcp);
      FS(4);
      unsigned spins = 0;
      for (; spins < SPIN_CAP; ++spins) {
        asm volatile("" ::: "memory");
        p0 = ld_sc1(rs, pb); p1 = ld_sc1(rs, pb + 1024); p2 = ld_sc1(rs, pb + 2048); p3 = ld_sc1(rs, pb + 3072); p4 = ld_sc1(rs, pb + 4096);
        const bool ok = !(isbad(p0) | isbad(p1) | isbad(p2) | isbad(p3) | isbad(p4));
        if (__all(ok)) break;
      }
      dcp += spins ? ct.up : -ct.dn;
      FS(5);
      f32x4 d = {0, 0, 0, 0}, d2 = {0, 0, 0, 0};
      d = mfma(ux[0], p0, d); d2 = mfma(ux[1], p1, d2); d = mfma(ux[2], p2, d); d2 = mfma(ux[3], p3, d2); d = mfma(um, p4, d);
      d += d2;
      if ((l16 < 8) == (g < 2)) *(f32x4*)(red + (((w * 2 + (g >> 1)) * 8 + (l16 & 7)) * 8 + (g & 1) * 4)) = d;
      FS(6);
      __syncthreads();
      tref = now();
      FS(7);
      if (tid == 64) set_flag(ws, s, 32 + nb);
      if (w == 0) {
        float r_[8];
#pragma unroll
        for (int q = 0; q < 8; ++q) r_[q] = red[q * 64 + lane];
        __builtin_amdgcn_sched_barrier(0);
        const float v = ((r_[0] + r_[1]) + (r_[2] + r_[3])) + ((r_[4] + r_[5]) + (r_[6] + r_[7]));
        const float vn = __builtin_bit_cast(float, __builtin_amdgcn_mov_dpp(__builtin_bit_cast(int, v), 0xB1, 0xF, 0xF, true));
        const unsigned d0 = desent(cvtpk(v, vn));
        const unsigned d1 = (unsigned)__builtin_amdgcn_mov_dpp((int)d0, 0x4E, 0xF, 0xF, true);
        const unsigned e0 = (unsigned)__builtin_amdgcn_mov_dpp((int)d0, 0x104, 0xF, 0xF, true);
        const unsigned e1 = (unsigned)__builtin_amdgcn_mov_dpp((int)d1, 0x104, 0xF, 0xF, true);
        if ((lane & 7) == 0)
          __builtin_amdgcn_raw_buffer_store_b128(v4u{d0, d1, e0, e1}, rs, (int)OFF_MEM + (s + 1) * 10240 + nb * 128 + (lane >> 3) * 16, 0, 16);
      }
      FS(8);
      FS_FLUSH();
    }
  }
}

__device__ __forceinline__ void item_pa(unsigned char* ws, rsrc_t rs, int w, int seg, int hg) {
  const int lane = lane_id(), g = lane >> 4, l16 = lane & 15;
  const float scale = 0.07216878364870322f * 1.4426950408889634f;
  v4u q[2][2][6], k[2][3][6];
  auto loadf = [&](int base, v4u (&o)[6]) {
#pragma unroll
    for (int gr = 0; gr < 4; ++gr) o[gr] = ld_sc1(rs, base + gr * 1536 + lane * 16);
#pragma unroll
    for (int pr = 0; pr < 2; ++pr) {
      const v2u a = ld8_sc1(rs, base + (2 * pr) * 1536 + 1024 + lane * 8), b = ld8_sc1(rs, base + (2 * pr + 1) * 1536 + 1024 + lane * 8);
      o[4 + pr] = v4u{a.x, a.y, b.x, b.y};
    }
  };
  wave_wait_count(ws, CNT_M + (seg >> 2), m_target(seg >> 2));
  if (seg < 8) wave_wait_count(ws, CNT_Q0 + (seg >> 2), 32);
#pragma unroll
  for (int hh = 0; hh < 2; ++hh) {
    const int h = 8 * hg + 2 * w + hh;
    const int qb = (int)OFF_QX + (seg * 32 + h) * 12288, kb = (int)OFF_KX + (seg * 32 + h) * 12288;
    loadf(qb, q[hh][0]); loadf(qb + 6144, q[hh][1]); loadf(kb, k[hh][1]); loadf(kb + 6144, k[hh][2]);
  }
  __builtin_amdgcn_sched_barrier(0);
  wave_wait_flags(ws, seg, 8 * hg + 2 * w, 2, lane);
#pragma unroll
  for (int hh = 0; hh < 2; ++hh) loadf((int)OFF_KM + (seg * 32 + 8 * hg + 2 * w + hh) * 6144, k[hh][0]);
  __builtin_amdgcn_sched_barrier(0);
  const size_t pxb = OFF_PX + (size_t)seg * 81920;
#pragma unroll
  for (int hh = 0; hh < 2; ++hh) {
    const int h = 8 * hg + 2 * w + hh;
#pragma unroll
    for (int T = 0; T < 2; ++T) {
      f32x4 t[3];
#pragma unroll
      for (int kt = 2; kt >= 0; --kt) {
        t[kt] = f32x4{0, 0, 0, 0};
#pragma unroll
        for (int f = 0; f < 6; ++f) t[kt] = mfma(k[hh][kt][f], q[hh][T][f], t[kt]);
      }
      const int tq = 16 * T + l16;
      const float NEG = -1e30f;
      float v[12];
#pragma unroll
      for (int r = 0; r < 4; ++r) {
        v[r] = g < 2 ? t[0][r] * scale : NEG;
        v[4 + r] = (4 * g + r <= tq) ? t[1][r] * scale : NEG;
        v[8 + r] = (16 + 4 * g + r <= tq) ? t[2][r] * scale : NEG;
      }
      float mx = v[0];
#pragma unroll
      for (int i = 1; i < 12; ++i) mx = fmaxf(mx, v[i]);
      mx = xmax32(xmax16(mx));
      float sum = 0.f;
#pragma unroll
      for (int i = 0; i < 12; ++i) { v[i] = __builtin_amdgcn_exp2f(v[i] - mx); sum += v[i]; }
      sum = xadd32(xadd16(sum));
      const float inv = __builtin_amdgcn_rcpf(sum);
      v4u px = {cvtpk(v[4] * inv, v[5] * inv), cvtpk(v[6] * inv, v[7] * inv), cvtpk(v[8] * inv, v[9] * inv), cvtpk(v[10] * inv, v[11] * inv)};
      st16(rs, pxb + (h * 2 + T) * 1024 + lane * 16, px);
      if (g < 2) {
        v2u pm = {cvtpk(v[0] * inv, v[1] * inv), cvtpk(v[2] * inv, v[3] * inv)};
        st8(rs, pxb + 65536 + ((h >> 2) * 2 + T) * 1024 + ((h & 3) * 16 + l16) * 16 + g * 8, pm);
      }
    }
  }
}

__device__ __forceinline__ void item_pb(const u16* x, unsigned char* ws, rsrc_t rs, unsigned char* smem, int w, int seg, int part) {
  const int lane = lane_id(), g = lane >> 4, l16 = lane & 15;
  const int pxb = (int)OFF_PX + seg * 81920 + lane * 16;
  v4u pf[10][2], uf[10][4];
  wave_wait_count(ws, CNT_M + (seg >> 2), m_target(seg >> 2));
#pragma unroll
  for (int i = 0; i < 8; ++i) {
    const int h = 8 * w + i;
#pragma unroll
    for (int c = 0; c < 4; ++c)
      uf[i][c] = ld_sc1(rs, (int)OFF_UX + ((seg * 80 + 2 * (part * 4 + c) + (l16 >> 3)) * 16 + (h >> 1)) * 1024 + (g * 16 + (h & 1) * 8 + (l16 & 7)) * 16);
  }
  const int pr = w >> 1, Tf = w & 1;
  v2u xv[2];
#pragma unroll
  for (int i = 0; i < 2; ++i) xv[i] = *(const v2u*)(x + (size_t)(seg * 32 + 16 * Tf + l16) * DM + 16 * (part * 4 + 2 * pr + i) + 4 * g);
  __builtin_amdgcn_sched_barrier(0);
  wave_wait_count(ws, CNT_PA + seg, 4);
  wave_wait_flags(ws, seg, 32 + 8 * part, 8, lane);
#pragma unroll
  for (int i = 0; i < 8; ++i)
#pragma unroll
    for (int T = 0; T < 2; ++T) pf[i][T] = ld_sc1(rs, pxb + ((8 * w + i) * 2 + T) * 1024);
#pragma unroll
  for (int i = 0; i < 2; ++i) {
    const int hq = 2 * w + i;
#pragma unroll
    for (int T = 0; T < 2; ++T) pf[8 + i][T] = ld_sc1(rs, pxb + 65536 + (hq * 2 + T) * 1024);
#pragma unroll
    for (int c = 0; c < 4; ++c)
      uf[8 + i][c] = ld_sc1(rs, (int)OFF_UM + ((seg * 32 + 4 * hq + g) * 640 + 16 * (part * 4 + c) + l16) * 16);
  }
  __builtin_amdgcn_sched_barrier(0);
  f32x4 acc[4][2];
#pragma unroll
  for (int c = 0; c < 4; ++c) { acc[c][0] = f32x4{0, 0, 0, 0}; acc[c][1] = f32x4{0, 0, 0, 0}; }
#pragma unroll
  for (int i = 0; i < 10; ++i)
#pragma unroll
    for (int c = 0; c < 4; ++c) {
      acc[c][0] = mfma(uf[i][c], pf[i][0], acc[c][0]);
      acc[c][1] = mfma(uf[i][c], pf[i][1], acc[c][1]);
    }
  f32x4* red = (f32x4*)smem;
#pragma unroll
  for (int c = 0; c < 4; ++c)
#pragma unroll
    for (int T = 0; T < 2; ++T) red[((w * 4 + c) * 2 + T) * 64 + lane] = acc[c][T];
  __syncthreads();
  {
    f32x4 o[2];
#pragma unroll
    for (int i = 0; i < 2; ++i) {
      const int c = 2 * pr + i;
      o[i] = (red[((0 * 4 + c) * 2 + Tf) * 64 + lane] + red[((1 * 4 + c) * 2 + Tf) * 64 + lane]) +
             (red[((2 * 4 + c) * 2 + Tf) * 64 + lane] + red[((3 * 4 + c) * 2 + Tf) * 64 + lane]);
      o[i].x = bflo(xv[i].x) + rbf(o[i].x); o[i].y = bfhi(xv[i].x) + rbf(o[i].y);
      o[i].z = bflo(xv[i].y) + rbf(o[i].z); o[i].w = bfhi(xv[i].y) + rbf(o[i].w);
    }
    st16(rs, OFF_HF + ((size_t)(seg * 2 + Tf) * KS_D + part * 2 + pr) * 1024 + lane * 16, pack8(o[0], o[1]));
  }
}

__device__ __forceinline__ float gelu_tanh(float x) {
  const float u = 0.7978845608028654f * (x + 0.044715f * x * x * x);
  const float e = __expf(2.f * u);
  const float t = 1.f - 2.f / (1.f + e);
  return 0.5f * x * (1.f + t);
}

__device__ __forceinline__ void item_f1(unsigned char* ws, rsrc_t rs, unsigned char* smem, int w, int seg, int part) {
  const int lane = lane_id(), g = lane >> 4, l16 = lane & 15;
  const unsigned char* wg = ws + OFF_WGF + (size_t)(part * 8 + 2 * w) * KS_D * 1024 + lane * 16;
  const unsigned char* wu = ws + OFF_WUF + (size_t)(part * 8 + 2 * w) * KS_D * 1024 + lane * 16;
  v4u wf[KS_D][4];
#pragma unroll
  for (int ks = 0; ks < KS_D; ++ks) {
    wf[ks][0] = *(const v4u*)(wg + ks * 1024); wf[ks][1] = *(const v4u*)(wg + (KS_D + ks) * 1024);
    wf[ks][2] = *(const v4u*)(wu + ks * 1024); wf[ks][3] = *(const v4u*)(wu + (KS_D + ks) * 1024);
  }
  __builtin_amdgcn_sched_barrier(0);
  wave_wait_count(ws, CNT_PB + seg, 10);
  v4u hv[2][5];
#pragma unroll
  for (int T = 0; T < 2; ++T)
#pragma unroll
    for (int i = 0; i < 5; ++i) hv[T][i] = ld_sc1(rs, (int)OFF_HF + ((seg * 2 + T) * KS_D + 5 * w + i) * 1024 + lane * 16);
  __builtin_amdgcn_sched_barrier(0);
  float* lred = (float*)(smem + 40960);
  float mean[2], rstd[2];
#pragma unroll
  for (int T = 0; T < 2; ++T) {
    float sacc = 0.f;
#pragma unroll
    for (int i = 0; i < 5; ++i) {
      const v4u u = hv[T][i];
      sacc += (bflo(u.x) + bfhi(u.x)) + (bflo(u.y) + bfhi(u.y)) + (bflo(u.z) + bfhi(u.z)) + (bflo(u.w) + bfhi(u.w));
    }
    sacc = xadd32(xadd16(sacc));
    if (g == 0) lred[w * 32 + 16 * T + l16] = sacc;
  }
  __syncthreads();
#pragma unroll
  for (int T = 0; T < 2; ++T) {
    mean[T] = ((lred[16 * T + l16] + lred[32 + 16 * T + l16]) + (lred[64 + 16 * T + l16] + lred[96 + 16 * T + l16])) * (1.f / DM);
    float q = 0.f;
#pragma unroll
    for (int i = 0; i < 5; ++i) {
      const unsigned u[4] = {hv[T][i].x, hv[T][i].y, hv[T][i].z, hv[T][i].w};
#pragma unroll
      for (int j = 0; j < 4; ++j) { const float d0 = bflo(u[j]) - mean[T], d1 = bfhi(u[j]) - mean[T]; q += d0 * d0 + d1 * d1; }
    }
    q = xadd32(xadd16(q));
    if (g == 0) lred[128 + w * 32 + 16 * T + l16] = q;
  }
  __syncthreads();
#pragma unroll
  for (int T = 0; T < 2; ++T) {
    const float vv = ((lred[128 + 16 * T + l16] + lred[160 + 16 * T + l16]) + (lred[192 + 16 * T + l16] + lred[224 + 16 * T + l16])) * (1.f / DM);
    rstd[T] = rsqrtf(vv + 1e-5f);
#pragma unroll
    for (int i = 0; i < 5; ++i) {
      const unsigned u[4] = {hv[T][i].x, hv[T][i].y, hv[T][i].z, hv[T][i].w};
      v4u o;
      o.x = cvtpk((bflo(u[0]) - mean[T]) * rstd[T], (bfhi(u[0]) - mean[T]) * rstd[T]);
      o.y = cvtpk((bflo(u[1]) - mean[T]) * rstd[T], (bfhi(u[1]) - mean[T]) * rstd[T]);
      o.z = cvtpk((bflo(u[2]) - mean[T]) * rstd[T], (bfhi(u[2]) - mean[T]) * rstd[T]);
      o.w = cvtpk((bflo(u[3]) - mean[T]) * rstd[T], (bfhi(u[3]) - mean[T]) * rstd[T]);
      *(v4u*)(smem + (T * KS_D + 5 * w + i) * 1024 + lane * 16) = o;
    }
  }
  __syncthreads();
  f32x4 acc[4][2];
#pragma unroll
  for (int a = 0; a < 4; ++a) { acc[a][0] = f32x4{0, 0, 0, 0}; acc[a][1] = f32x4{0, 0, 0, 0}; }
#pragma unroll
  for (int ks = 0; ks < KS_D; ++ks) {
    const v4u b0 = *(const v4u*)(smem + ks * 1024 + lane * 16), b1 = *(const v4u*)(smem + (KS_D + ks) * 1024 + lane * 16);
#pragma unroll
    for (int a = 0; a < 4; ++a) { acc[a][0] = mfma(wf[ks][a], b0, acc[a][0]); acc[a][1] = mfma(wf[ks][a], b1, acc[a][1]); }
  }
#pragma unroll
  for (int T = 0; T < 2; ++T) {
    f32x4 o[2];
#pragma unroll
    for (int i = 0; i < 2; ++i)
#pragma unroll
      for (int r = 0; r < 4; ++r) {
        const float gg = rbf(acc[i][T][r]), uu = rbf(acc[2 + i][T][r]);
        o[i][r] = rbf(gelu_tanh(gg)) * uu;
      }
    st16(rs, OFF_ACTF + ((size_t)(seg * 2 + T) * KS_F + part * 4 + w) * 1024 + lane * 16, pack8(o[0], o[1]));
  }
}

__device__ __forceinline__ void item_f2(unsigned char* ws, rsrc_t rs, unsigned char* smem, u16* out, int w, int seg, int part) {
  const int lane = lane_id(), g = lane >> 4, l16 = lane & 15;
  v4u af[12][4], bf[12][2];
#pragma unroll
  for (int i = 0; i < 12; ++i)
#pragma unroll
    for (int c = 0; c < 4; ++c) af[i][c] = *(const v4u*)(ws + OFF_WDF + ((size_t)(part * 4 + c) * KS_F + 12 * w + i) * 1024 + lane * 16);
  __builtin_amdgcn_sched_barrier(0);
  wave_wait_count(ws, CNT_F1 + seg, 12);
#pragma unroll
  for (int i = 0; i < 12; ++i)
#pragma unroll
    for (int T = 0; T < 2; ++T) bf[i][T] = ld_sc1(rs, (int)OFF_ACTF + ((seg * 2 + T) * KS_F + 12 * w + i) * 1024 + lane * 16);
  const int pr = w >> 1, Tf = w & 1;
  const v4u hres = ld_sc1(rs, (int)OFF_HF + ((seg * 2 + Tf) * KS_D + part * 2 + pr) * 1024 + lane * 16);
  __builtin_amdgcn_sched_barrier(0);
  f32x4 acc[4][2];
#pragma unroll
  for (int c = 0; c < 4; ++c) { acc[c][0] = f32x4{0, 0, 0, 0}; acc[c][1] = f32x4{0, 0, 0, 0}; }
#pragma unroll
  for (int i = 0; i < 12; ++i)
#pragma unroll
    for (int c = 0; c < 4; ++c) { acc[c][0] = mfma(af[i][c], bf[i][0], acc[c][0]); acc[c][1] = mfma(af[i][c], bf[i][1], acc[c][1]); }
  f32x4* red = (f32x4*)smem;
#pragma unroll
  for (int c = 0; c < 4; ++c)
#pragma unroll
    for (int T = 0; T < 2; ++T) red[((w * 4 + c) * 2 + T) * 64 + lane] = acc[c][T];
  __syncthreads();
  {
    const unsigned hr[4] = {hres.x, hres.y, hres.z, hres.w};
    const int tok = seg * 32 + 16 * Tf + l16;
#pragma unroll
    for (int i = 0; i < 2; ++i) {
      const int c = 2 * pr + i;
      f32x4 o = (red[((0 * 4 + c) * 2 + Tf) * 64 + lane] + red[((1 * 4 + c) * 2 + Tf) * 64 + lane]) +
                (red[((2 * 4 + c) * 2 + Tf) * 64 + lane] + red[((3 * 4 + c) * 2 + Tf) * 64 + lane]);
      o.x = bflo(hr[2 * i]) + rbf(o.x); o.y = bfhi(hr[2 * i]) + rbf(o.y);
      o.z = bflo(hr[2 * i + 1]) + rbf(o.z); o.w = bfhi(hr[2 * i + 1]) + rbf(o.w);
      *(v2u*)(out + (size_t)tok * DM + 16 * (part * 4 + c) + 4 * g) = pack4(o);
    }
  }
  __syncthreads();
}

constexpr int NCHAIN = NH + NBK, NWORK = 256 - NCHAIN;
constexpr int IT_QKR = 448, IT_PRE = 96 + IT_QKR + 64, IT_ROUND = 36, N_ROUNDS = NSEG + 3;
constexpr int IT_TOTAL = IT_PRE + N_ROUNDS * IT_ROUND;
struct MainArgs { const u16* x; const u16* wv; unsigned char* ws; u16* out; ChainT ct; };

__global__ void __launch_bounds__(256, 1) k_main(MainArgs a) {
  extern __shared__ __attribute__((aligned(16))) unsigned char smem[];
  unsigned char* ws = a.ws;
  const rsrc_t rs = __builtin_amdgcn_make_buffer_rsrc((void*)ws, 0, (int)WS_TOTAL, 0x00020000);
  TL(100);
  if (blockIdx.x < NCHAIN) { chain_role(a.wv, ws, rs, smem, a.ct); return; }
  const int wk = blockIdx.x - NCHAIN;
  const int w = __builtin_amdgcn_readfirstlane(threadIdx.x >> 6);
  if (wk < 80) {
    item_wvo(a.wv, ws, rs, smem, w, (80 + wk) / 5, (80 + wk) % 5);
    signal_done(ws, w, CNT_WVO);
  } else {
    item_piece(ws, rs, smem, w, 0, (wk - 80) & 31, 2 + ((wk - 80) >> 5));
    signal_done(ws, w, CNT_M);
  }
  TL(110);
  int it = wk;
  for (; it < IT_PRE; it += NWORK) {
    if (it < 96) {
      item_piece(ws, rs, smem, w, 1, it & 31, it < 64 ? 2 + (it >> 5) : 1);
      signal_done(ws, w, CNT_M + 1);
    } else if (it < 96 + IT_QKR) {
      const int j = it - 96 + 64;
      item_qkvu(ws, rs, smem, w, j >> 5, j & 31);
      signal_done(ws, w, CNT_M + (j >> 5));
    } else {
      const int j = it - 96 - IT_QKR;
      item_piece(ws, rs, smem, w, j >> 5, j & 31, 0);
      signal_done(ws, w, CNT_Q0 + (j >> 5));
    }
    TL(111 + it / NWORK);
  }
  TL(101);
  for (; it < IT_TOTAL; it += NWORK) {
    const int j = it - IT_PRE, r = j / IT_ROUND, q = j % IT_ROUND;
    TL(j / NWORK);
    if (q < 4) {
      const int seg = r;
      if (seg >= NSEG) continue;
      item_pa(ws, rs, w, seg, q);
      signal_done(ws, w, CNT_PA + seg);
    } else if (q < 14) {
      const int seg = r - 1;
      if (seg < 0 || seg >= NSEG) continue;
      item_pb(a.x, ws, rs, smem, w, seg, q - 4);
      signal_done(ws, w, CNT_PB + seg);
    } else if (q < 26) {
      const int seg = r - 2;
      if (seg < 0 || seg >= NSEG) continue;
      item_f1(ws, rs, smem, w, seg, q - 14);
      signal_done(ws, w, CNT_F1 + seg);
    } else {
      const int seg = r - 3;
      if (seg < 0 || seg >= NSEG) continue;
      item_f2(ws, rs, smem, a.out, w, seg, q - 26);
    }
  }
  TL(102);
}

extern "C" void kernel_launch(void* const* d_in, const int* in_sizes, int n_in, void* d_out, int out_size, void* d_ws,
                              size_t ws_size, hipStream_t stream) {
  unsigned char* ws = (unsigned char*)d_ws;
  PrepArgs pa;
  pa.x = (const u16*)d_in[0]; pa.mem0 = (const u16*)d_in[1]; pa.wq = (const u16*)d_in[2]; pa.wk = (const u16*)d_in[3];
  pa.wv = (const u16*)d_in[4]; pa.wo = (const u16*)d_in[5]; pa.wg = (const u16*)d_in[8]; pa.wu = (const u16*)d_in[9];
  pa.wd = (const u16*)d_in[10]; pa.ws = ws;
  constexpr int LDS_MAIN = 136 * 1024;
  (void)hipFuncSetAttribute((const void*)k_main, hipFuncAttributeMaxDynamicSharedMemorySize, LDS_MAIN);
  (void)hipMemsetAsync(ws + OFF_MEM, 0xFF, (size_t)65 * 10240 + (size_t)64 * 20480, stream);
  (void)hipMemsetAsync(ws + OFF_CNT, 0, CTRL_BYTES, stream);
  k_prep<<<PREP_BLOCKS, 256, 0, stream>>>(pa);
  MainArgs ma;
  ma.x = pa.x; ma.wv = pa.wv; ma.ws = ws; ma.out = (u16*)d_out;
  ma.ct.dq0 = DQ0; ma.ct.dcm0 = DCM0; ma.ct.dcp0 = DCP0; ma.ct.dn = T_DN; ma.ct.up = T_UP; ma.ct.pad = 0;
  k_main<<<256, 256, LDS_MAIN, stream>>>(ma);
}
```

```cpp
#include <hip/hip_runtime.h>
#include <hip/hip_bf16.h>
#include <stdint.h>

constexpr int SEQ = 2048, DM = 640, NH = 32, DH = 192, NM = 8, SEG = 32, NSEG = 64, DF = 1536;
constexpr int KS_D = DM / 32;
constexpr int KS_F = DF / 32;
constexpr int NBK = 80;

constexpr size_t OFF_XNF  = 0;
constexpr size_t OFF_WQF  = OFF_XNF  + (size_t)128 * 20 * 1024;
constexpr size_t OFF_WKF  = OFF_WQF  + (size_t)384 * 20 * 1024;
constexpr size_t OFF_WVF  = OFF_WKF  + (size_t)384 * 20 * 1024;
constexpr size_t OFF_WOF  = OFF_WVF  + (size_t)384 * 20 * 1024;
constexpr size_t OFF_WGF  = OFF_WOF  + (size_t)32 * 40 * 6 * 1024;
constexpr size_t OFF_WUF  = OFF_WGF  + (size_t)96 * 20 * 1024;
constexpr size_t OFF_WDF  = OFF_WUF  + (size_t)96 * 20 * 1024;
constexpr size_t OFF_WVOF = OFF_WDF  + (size_t)40 * 48 * 1024;
constexpr size_t OFF_QX   = OFF_WVOF + (size_t)80 * 16 * 20 * 1024;
constexpr size_t OFF_KX   = OFF_QX   + (size_t)64 * 32 * 12288;
constexpr size_t OFF_UX   = OFF_KX   + (size_t)64 * 32 * 12288;
constexpr size_t OFF_MEM  = OFF_UX   + (size_t)64 * 80 * 16 * 1024;
constexpr size_t OFF_P    = OFF_MEM  + (size_t)65 * 10240;
constexpr size_t OFF_KM   = OFF_P    + (size_t)64 * 20480;
constexpr size_t OFF_UM   = OFF_KM   + (size_t)64 * 32 * 6144;
constexpr size_t OFF_PX   = OFF_UM   + (size_t)64 * 32 * 10240;
constexpr size_t OFF_HF   = OFF_PX   + (size_t)64 * 81920;
constexpr size_t OFF_ACTF = OFF_HF   + (size_t)128 * 20 * 1024;
constexpr size_t OFF_CNT  = OFF_ACTF + (size_t)128 * 48 * 1024;
constexpr int CNT_WVO = 0, CNT_M = 1, CNT_SEG = 17, CNT_PA = 81, CNT_PB = 145, CNT_F1 = 209, CNT_Q0 = 273, CNT_N = 275;
__host__ __device__ constexpr unsigned m_target(int m) { return m <= 1 ? 96u : 32u; }
constexpr size_t OFF_FLG  = OFF_CNT + (size_t)CNT_N * 256;
constexpr size_t CTRL_BYTES = (size_t)CNT_N * 256 + 64 * 512;
constexpr size_t WS_TOTAL = OFF_CNT + CTRL_BYTES + 256;
static_assert(WS_TOTAL < (size_t)256 * 1024 * 1024, "ws too big");

typedef short bf16x8 __attribute__((ext_vector_type(8)));
typedef float f32x4 __attribute__((ext_vector_type(4)));
typedef unsigned v4u __attribute__((ext_vector_type(4)));
typedef unsigned v2u __attribute__((ext_vector_type(2)));
typedef unsigned short u16;

__device__ __forceinline__ f32x4 mfma(v4u a, v4u b, f32x4 c) {
  return __builtin_amdgcn_mfma_f32_16x16x32_bf16(__builtin_bit_cast(bf16x8, a), __builtin_bit_cast(bf16x8, b), c, 0, 0, 0);
}
typedef __bf16 bf16x2_t __attribute__((ext_vector_type(2)));
typedef float f32x2 __attribute__((ext_vector_type(2)));
__device__ __forceinline__ unsigned cvtpk(float lo, float hi) {
  f32x2 v = {lo, hi};
  return __builtin_bit_cast(unsigned, __builtin_convertvector(v, bf16x2_t));
}
__device__ __forceinline__ float bf2f(unsigned short b) { return __builtin_bit_cast(float, (unsigned)b << 16); }
__device__ __forceinline__ float bflo(unsigned u) { return __builtin_bit_cast(float, u << 16); }
__device__ __forceinline__ float bfhi(unsigned u) { return __builtin_bit_cast(float, u & 0xffff0000u); }
__device__ __forceinline__ float rbf(float x) { return bflo(cvtpk(x, x)); }
__device__ __forceinline__ v4u pack8(f32x4 a, f32x4 b) {
  v4u r; r.x = cvtpk(a.x, a.y); r.y = cvtpk(a.z, a.w); r.z = cvtpk(b.x, b.y); r.w = cvtpk(b.z, b.w); return r;
}
__device__ __forceinline__ v2u pack4(f32x4 a) { v2u r; r.x = cvtpk(a.x, a.y); r.y = cvtpk(a.z, a.w); return r; }
constexpr unsigned SENT = 0xFFFFFFFFu;
__device__ __forceinline__ unsigned desent(unsigned x) { return x == SENT ? 0x7FC07FC0u : x; }
__device__ __forceinline__ bool isbad(v4u v) { return (v.x == SENT) | (v.y == SENT) | (v.z == SENT) | (v.w == SENT); }

#define RAW_BARRIER() do { asm volatile("s_waitcnt lgkmcnt(0)" ::: "memory"); __builtin_amdgcn_s_barrier(); } while (0)
#define WAIT_V(n) asm volatile("s_waitcnt vmcnt(" #n ")" ::: "memory")

__device__ __forceinline__ void glds16(const unsigned char* g, unsigned char* l) {
  __builtin_amdgcn_global_load_lds((const unsigned*)g, (unsigned*)l, 16, 0, 0);
}

typedef __amdgpu_buffer_rsrc_t rsrc_t;
__device__ __forceinline__ int lane_id() {
  int l; asm volatile("v_mbcnt_lo_u32_b32 %0, -1, 0\n\tv_mbcnt_hi_u32_b32 %0, -1, %0" : "=v"(l)); return l;
}
__device__ __forceinline__ void st16(rsrc_t rs, size_t off, v4u v) { __builtin_amdgcn_raw_buffer_store_b128(v, rs, (int)off, 0, 16); }
__device__ __forceinline__ void st8(rsrc_t rs, size_t off, v2u v) { __builtin_amdgcn_raw_buffer_store_b64(v, rs, (int)off, 0, 16); }
__device__ __forceinline__ unsigned* cnt_ptr(unsigned char* ws, int i) { return (unsigned*)(ws + OFF_CNT + (size_t)i * 256); }
constexpr unsigned SPIN_CAP = 1u << 22;
#define POLL_FAST 4
#define FAST_SEG 63
#define POLL_SLEEP 64
__device__ __forceinline__ void signal_done(unsigned char* ws, int w, int i) {
  asm volatile("s_waitcnt vmcnt(0)" ::: "memory");
  __syncthreads();
  if (w == 0 && lane_id() == 0) __hip_atomic_fetch_add(cnt_ptr(ws, i), 1u, __ATOMIC_RELAXED, __HIP_MEMORY_SCOPE_AGENT);
}
__device__ __forceinline__ void wait_count(unsigned char* ws, int w, int i, unsigned target) {
  if (w == 0 && lane_id() == 0) {
    unsigned* p = cnt_ptr(ws, i);
    for (unsigned spins = 0; spins < SPIN_CAP; ++spins) {
      if (__hip_atomic_load(p, __ATOMIC_RELAXED, __HIP_MEMORY_SCOPE_AGENT) >= target) break;
      __builtin_amdgcn_s_sleep(2);
    }
    __builtin_amdgcn_fence(__ATOMIC_ACQUIRE, "agent");
    asm volatile("s_waitcnt vmcnt(0)" ::: "memory");
  }
  __syncthreads();
}
__device__ __forceinline__ void wave_wait_count(unsigned char* ws, int i, unsigned target, bool fast = false) {
  unsigned* p = cnt_ptr(ws, i);
  for (unsigned spins = 0; spins < SPIN_CAP; ++spins) {
    if (__builtin_amdgcn_readfirstlane(__hip_atomic_load(p, __ATOMIC_RELAXED, __HIP_MEMORY_SCOPE_AGENT)) >= target) break;
    if (fast) __builtin_amdgcn_s_sleep(POLL_FAST); else __builtin_amdgcn_s_sleep(POLL_SLEEP);
  }
  __builtin_amdgcn_fence(__ATOMIC_ACQUIRE, "wavefront");
}
__device__ __forceinline__ void set_flag(unsigned char* ws, int seg, int i) {
  __hip_atomic_store((unsigned*)(ws + OFF_FLG + (size_t)seg * 512) + i, 1u, __ATOMIC_RELAXED, __HIP_MEMORY_SCOPE_AGENT);
}
__device__ __forceinline__ void wave_wait_flags(unsigned char* ws, int seg, int i0, int n, int lane, bool fast = false) {
  unsigned* p = (unsigned*)(ws + OFF_FLG + (size_t)seg * 512) + i0 + (lane < n ? lane : 0);
  for (unsigned spins = 0; spins < SPIN_CAP; ++spins) {
    if (__all(__hip_atomic_load(p, __ATOMIC_RELAXED, __HIP_MEMORY_SCOPE_AGENT) != 0u)) break;
    if (fast) __builtin_amdgcn_s_sleep(POLL_FAST); else __builtin_amdgcn_s_sleep(POLL_SLEEP);
  }
  __builtin_amdgcn_fence(__ATOMIC_ACQUIRE, "wavefront");
}

__device__ __forceinline__ void swap16(float v, float& a, float& b) {
  a = v; b = v;
  asm volatile("s_nop 1\n\tv_permlane16_swap_b32 %0, %1\n\ts_nop 1" : "+v"(a), "+v"(b));
}
__device__ __forceinline__ void swap32(float v, float& a, float& b) {
  a = v; b = v;
  asm volatile("s_nop 1\n\tv_permlane32_swap_b32 %0, %1\n\ts_nop 1" : "+v"(a), "+v"(b));
}
__device__ __forceinline__ float xmax16(float v) { float a, b; swap16(v, a, b); return fmaxf(a, b); }
__device__ __forceinline__ float xmax32(float v) { float a, b; swap32(v, a, b); return fmaxf(a, b); }
__device__ __forceinline__ float xadd16(float v) { float a, b; swap16(v, a, b); return a + b; }
__device__ __forceinline__ float xadd32(float v) { float a, b; swap32(v, a, b); return a + b; }
__device__ __forceinline__ v4u ld_sc1(__amdgpu_buffer_rsrc_t rs, int off) { return __builtin_amdgcn_raw_buffer_load_b128(rs, off, 0, 16); }
__device__ __forceinline__ v2u ld8_sc1(__amdgpu_buffer_rsrc_t rs, int off) { return __builtin_amdgcn_raw_buffer_load_b64(rs, off, 0, 16); }
__device__ __forceinline__ void swapu16(unsigned& a, unsigned& b) { asm volatile("s_nop 1\n\tv_permlane16_swap_b32 %0, %1\n\ts_nop 1" : "+v"(a), "+v"(b)); }

struct PrepArgs {
  const u16* x; const u16* mem0; const u16* wq; const u16* wk; const u16* wv; const u16* wo;
  const u16* wg; const u16* wu; const u16* wd; unsigned char* ws;
};

__device__ __forceinline__ void relayout_tile(const u16* src, int ld, int ks, int n0, int ncols, unsigned char* dst, size_t frag0, size_t frag_stride,
                                              bool perm, unsigned char* tile) {
  constexpr int RS = 528;
  const int t = threadIdx.x;
#pragma unroll
  for (int i = 0; i < 4; ++i) {
    const int id = t + 256 * i, row = id >> 5, c8 = id & 31;
    if (c8 * 8 < ncols) *(v4u*)(tile + row * RS + c8 * 16) = *(const v4u*)(src + (size_t)(ks * 32 + row) * ld + n0 + c8 * 8);
  }
  __syncthreads();
  const int lane = t & 63, w = t >> 6, g = lane >> 4, q = (lane >> 2) & 3, p = lane & 3;
  const int r0 = (perm ? 4 * g : 8 * g) + q, r1 = (perm ? 16 + 4 * g : 8 * g + 4) + q;
  const unsigned base = (unsigned)(size_t)tile;
#pragma unroll
  for (int i = 0; i < 4; ++i) {
    const int f = w * 4 + i;
    if (f * 16 < ncols) {
      v2u lo, hi;
      asm volatile("ds_read_b64_tr_b16 %0, %2\n\tds_read_b64_tr_b16 %1, %3\n\ts_waitcnt lgkmcnt(0)"
                   : "=&v"(lo), "=&v"(hi) : "v"(base + r0 * RS + f * 32 + p * 8), "v"(base + r1 * RS + f * 32 + p * 8) : "memory");
      *(v4u*)(dst + (frag0 + (size_t)f * frag_stride) * 1024 + lane * 16) = v4u{lo.x, lo.y, hi.x, hi.y};
    }
  }
}

constexpr int PREP_LN = 128, PREP_WQKV = 20 * 24, PREP_WO = 192 * 3, PREP_WGU = 20 * 6, PREP_WD = 48 * 3, PREP_FILL = 64;
constexpr int PREP_BLOCKS = PREP_LN + 3 * PREP_WQKV + PREP_WO + 2 * PREP_WGU + PREP_WD + PREP_FILL;

__global__ void __launch_bounds__(256) k_prep(PrepArgs a) {
  __shared__ __attribute__((aligned(16))) unsigned char tile[32 * 528];
  int b = blockIdx.x;
  const int t = threadIdx.x;
  if (b < 3 * PREP_WQKV) {
    const int which = b % 3, r = b / 3, ks = r % 20, nc = r / 20;
    const u16* src = which == 0 ? a.wk : (which == 1 ? a.wv : a.wq);
    unsigned char* dst = a.ws + (which == 0 ? OFF_WKF : (which == 1 ? OFF_WVF : OFF_WQF));
    relayout_tile(src, NH * DH, ks, nc * 256, 256, dst, (size_t)nc * 16 * KS_D + ks, KS_D, false, tile);
    return;
  }
  b -= 3 * PREP_WQKV;
  if (b < PREP_WO) {
    const int kk = b / 3, nc = b % 3;
    relayout_tile(a.wo, DM, kk, nc * 256, nc == 2 ? 128 : 256, a.ws + OFF_WOF, ((size_t)(kk / 6) * 40 + nc * 16) * 6 + kk % 6, 6, true, tile);
    return;
  }
  b -= PREP_WO;
  if (b < PREP_LN) {
    const int lane = t & 63, w = t >> 6, r = lane >> 4, c = lane & 15;
    const int row = b * 16 + w * 4 + r;
    v4u v[5];
    float s = 0.f;
#pragma unroll
    for (int i = 0; i < 5; ++i) {
      v[i] = *(const v4u*)(a.x + (size_t)row * DM + (c + 16 * i) * 8);
      s += bflo(v[i].x) + bfhi(v[i].x) + bflo(v[i].y) + bfhi(v[i].y) + bflo(v[i].z) + bfhi(v[i].z) + bflo(v[i].w) + bfhi(v[i].w);
    }
#pragma unroll
    for (int o = 1; o < 16; o <<= 1) s += __shfl_xor(s, o);
    const float mean = s * (1.f / DM);
    float q = 0.f;
#pragma unroll
    for (int i = 0; i < 5; ++i) {
      const unsigned u[4] = {v[i].x, v[i].y, v[i].z, v[i].w};
#pragma unroll
      for (int j = 0; j < 4; ++j) { float d0 = bflo(u[j]) - mean, d1 = bfhi(u[j]) - mean; q += d0 * d0 + d1 * d1; }
    }
#pragma unroll
    for (int o = 1; o < 16; o <<= 1) q += __shfl_xor(q, o);
    const float rs = rsqrtf(q * (1.f / DM) + 1e-5f);
#pragma unroll
    for (int i = 0; i < 5; ++i) {
      const unsigned u[4] = {v[i].x, v[i].y, v[i].z, v[i].w};
      v4u o;
      o.x = cvtpk((bflo(u[0]) - mean) * rs, (bfhi(u[0]) - mean) * rs);
      o.y = cvtpk((bflo(u[1]) - mean) * rs, (bfhi(u[1]) - mean) * rs);
      o.z = cvtpk((bflo(u[2]) - mean) * rs, (bfhi(u[2]) - mean) * rs);
      o.w = cvtpk((bflo(u[3]) - mean) * rs, (bfhi(u[3]) - mean) * rs);
      const int ch = c + 16 * i;
      *(v4u*)(a.ws + OFF_XNF + ((size_t)(row >> 4) * KS_D + (ch >> 2)) * 1024 + ((ch & 3) * 16 + (row & 15)) * 16) = o;
    }
    return;
  }
  b -= PREP_LN;
  if (b < 2 * PREP_WGU) {
    const int which = b / PREP_WGU, r = b % PREP_WGU, ks = r % 20, nc = r / 20;
    relayout_tile(which ? a.wu : a.wg, DF, ks, nc * 256, 256, a.ws + (which ? OFF_WUF : OFF_WGF), (size_t)nc * 16 * KS_D + ks, KS_D, true, tile);
    return;
  }
  b -= 2 * PREP_WGU;
  if (b < PREP_WD) {
    const int ks = b / 3, nc = b % 3;
    relayout_tile(a.wd, DM, ks, nc * 256, nc == 2 ? 128 : 256, a.ws + OFF_WDF, (size_t)nc * 16 * KS_F + ks, KS_F, true, tile);
    return;
  }
  b -= PREP_WD;
  {
    constexpr int NSENT = (64 * 10240 + 64 * 20480) / 16;
    const v4u ones = {SENT, SENT, SENT, SENT}, zero = {0u, 0u, 0u, 0u};
    for (int i = b * 256 + t; i < NSENT; i += PREP_FILL * 256) *(v4u*)(a.ws + OFF_MEM + 10240 + (size_t)i * 16) = ones;
    for (int i = b * 256 + t; i < (int)(CTRL_BYTES / 16); i += PREP_FILL * 256) *(v4u*)(a.ws + OFF_CNT + (size_t)i * 16) = zero;
    if (b == 0) {
      for (int id = t; id < 640; id += 256) {
        const int nb = id >> 3, tok = id & 7;
        v4u v = *(const v4u*)(a.mem0 + tok * DM + nb * 8);
        v.x = desent(v.x); v.y = desent(v.y); v.z = desent(v.z); v.w = desent(v.w);
        *(v4u*)(a.ws + OFF_MEM + id * 16) = v;
      }
    }
  }
}

#define MFMA2_A(c0, c1, a, b0, b1) asm volatile("v_mfma_f32_16x16x32_bf16 %0, %2, %3, %0\n\tv_mfma_f32_16x16x32_bf16 %1, %2, %4, %1" \
      : "+a"(c0), "+a"(c1) : "v"(a), "v"(b0), "v"(b1))
#define MFMA2_V(c0, c1, a, b0, b1) asm volatile("v_mfma_f32_16x16x32_bf16 %0, %2, %3, %0\n\tv_mfma_f32_16x16x32_bf16 %1, %2, %4, %1" \
      : "+v"(c0), "+v"(c1) : "v"(a), "v"(b0), "v"(b1))
#define MFMA2B_V(c0, c1, a0, a1, b) asm volatile("v_mfma_f32_16x16x32_bf16 %0, %2, %4, %0\n\tv_mfma_f32_16x16x32_bf16 %1, %3, %4, %1" \
      : "+v"(c0), "+v"(c1) : "v"(a0), "v"(a1), "v"(b))
template <int N> __device__ __forceinline__ void wait_v() { asm volatile("s_waitcnt vmcnt(%0)" ::"n"(N) : "memory"); }

template <int NA>
__device__ __forceinline__ void proj_gemm(unsigned char* ws, unsigned char* smem, int w, int lane, int m, int h, unsigned wsel, f32x4 (&acc)[NA][2]) {
  constexpr int FPK = NA + 8, LPW = FPK / 4, BUFB = FPK * 1024, GI = NA / LPW;
#pragma unroll
  for (int a = 0; a < NA; ++a) { acc[a][0] = f32x4{0, 0, 0, 0}; acc[a][1] = f32x4{0, 0, 0, 0}; }
  unsigned soff[LPW];
#pragma unroll
  for (int i = 0; i < LPW; ++i) {
    const int ff = w * LPW + i;
    const unsigned base = NA == 12 ? wsel : (ff < 12 ? (unsigned)OFF_WQF : (ff < 24 ? (unsigned)OFF_WKF : (unsigned)OFF_WVF));
    const int et = ff % 12;
    const unsigned o = ff < NA ? base + (unsigned)(h * 12 + et) * (KS_D * 1024u) : (unsigned)OFF_XNF + (unsigned)(m * 8 + ff - NA) * (KS_D * 1024u);
    soff[i] = __builtin_amdgcn_readfirstlane(o);
  }
  const unsigned char* gl = ws + lane * 16;
#pragma unroll
  for (int st = 0; st < 3; ++st)
#pragma unroll
    for (int i = 0; i < LPW; ++i) glds16(gl + (soff[i] + (unsigned)st * 1024u), smem + st * BUFB + (w * LPW + i) * 1024);
  wait_v<2 * LPW>();
  RAW_BARRIER();
  v4u af[NA], b0, b1;
  {
    const unsigned char* buf = smem + lane * 16;
    b0 = *(const v4u*)(buf + (NA + 2 * w) * 1024); b1 = *(const v4u*)(buf + (NA + 2 * w + 1) * 1024);
#pragma unroll
    for (int a = 0; a < NA; ++a) af[a] = *(const v4u*)(buf + a * 1024);
  }
  int bn = 1;
#pragma unroll 1
  for (int i = 0; i < KS_D - 1; ++i) {
    wait_v<LPW>();
    RAW_BARRIER();
    const int bf = bn == 0 ? 2 : bn - 1;
    const unsigned nst = (unsigned)(i + 3 < KS_D ? i + 3 : KS_D - 1) * 1024u;
    const unsigned char* buf = smem + bn * BUFB + lane * 16;
    const v4u nb0 = *(const v4u*)(buf + (NA + 2 * w) * 1024), nb1 = *(const v4u*)(buf + (NA + 2 * w + 1) * 1024);
    asm volatile("s_nop 1");
#pragma unroll
    for (int a = 0; a < NA; ++a) {
      if (a < 32) MFMA2_A(acc[a][0], acc[a][1], af[a], b0, b1); else MFMA2_V(acc[a][0], acc[a][1], af[a], b0, b1);
      af[a] = *(const v4u*)(buf + a * 1024);
      if (a % GI == 1 && a / GI < LPW) glds16(gl + (soff[a / GI] + nst), smem + bf * BUFB + (w * LPW + a / GI) * 1024);
    }
    b0 = nb0; b1 = nb1;
    bn = bn == 2 ? 0 : bn + 1;
  }
  asm volatile("s_nop 1");
#pragma unroll
  for (int a = 0; a < NA; ++a) {
    if (a < 32) MFMA2_A(acc[a][0], acc[a][1], af[a], b0, b1); else MFMA2_V(acc[a][0], acc[a][1], af[a], b0, b1);
  }
  wait_v<0>();
  asm volatile("s_nop 7\n\ts_nop 7" ::: "memory");
#pragma unroll
  for (int a = 0; a < NA; ++a) { if (a < 32) asm volatile("" : "+a"(acc[a][0]), "+a"(acc[a][1])); else asm volatile("" : "+v"(acc[a][0]), "+v"(acc[a][1])); }
}

__device__ __forceinline__ void u_issue(unsigned char* ws, unsigned char* smem, int w, int lane, int h, int st, int buf) {
  const unsigned char* wo = ws + OFF_WOF + (size_t)h * 40 * 6 * 1024 + lane * 16;
#pragma unroll
  for (int i = 0; i < 6; ++i) glds16(wo + (size_t)(st * 24 + w * 6 + i) * 1024, smem + buf * 24576 + (w * 6 + i) * 1024);
}
template <int EPI, int PRE>
__device__ __forceinline__ void u_stage(unsigned char* ws, rsrc_t rs, unsigned char* smem, int w, int lane, int h, const v4u (&vf)[2][6],
                                        int st0, int nst, size_t obase) {
  wait_v<12 + PRE>();
  RAW_BARRIER();
  v4u bf[24];
#pragma unroll
  for (int i = 0; i < 24; ++i) bf[i] = *(const v4u*)(smem + i * 1024 + lane * 16);
  int bn = 1;
#pragma unroll 1
  for (int i0 = 0; i0 < nst; ++i0) {
    if (i0 == 0) { wait_v<6 + PRE>(); } else if (i0 == 1) { wait_v<10 + PRE>(); } else { wait_v<14>(); }
    RAW_BARRIER();
    const int bfree = bn == 0 ? 2 : bn - 1;
    u_issue(ws, smem, w, lane, h, st0 + (i0 + 3 < nst ? i0 + 3 : nst - 1), bfree);
    const unsigned char* buf = smem + bn * 24576 + lane * 16;
    f32x4 d0[4], d1[4];
#pragma unroll
    for (int i = 0; i < 4; ++i) { d0[i] = f32x4{0, 0, 0, 0}; d1[i] = f32x4{0, 0, 0, 0}; }
    asm volatile("s_nop 1" : "+v"(d0[0]), "+v"(d1[0]), "+v"(d0[1]), "+v"(d1[1]), "+v"(d0[2]), "+v"(d1[2]), "+v"(d0[3]), "+v"(d1[3]));
#pragma unroll
    for (int p = 0; p < 6; ++p)
#pragma unroll
      for (int i = 0; i < 4; ++i) {
        MFMA2B_V(d0[i], d1[i], vf[0][p], vf[1][p], bf[i * 6 + p]);
        bf[i * 6 + p] = *(const v4u*)(buf + (i * 6 + p) * 1024);
      }
    asm volatile("s_nop 7\n\ts_nop 1" : "+v"(d0[0]), "+v"(d1[0]), "+v"(d0[1]), "+v"(d1[1]), "+v"(d0[2]), "+v"(d1[2]), "+v"(d0[3]), "+v"(d1[3]));
    const int nt0 = (st0 + i0) * 4;
#pragma unroll
    for (int i = 0; i < 4; ++i) {
      if (EPI == 0) {
        st16(rs, obase + (size_t)(nt0 + i) * 2 * 16 * 1024, pack8(d0[i], d1[i]));
      } else {
        const v2u a = pack4(d0[i]), b = pack4(d1[i]);
        unsigned ax = a.x, ay = a.y, bx = b.x, by = b.y;
        swapu16(ax, bx); swapu16(ay, by);
        st16(rs, obase + (size_t)(nt0 + i) * 2 * 16 * 20 * 1024, v4u{ax, ay, bx, by});
      }
    }
    bn = bn == 2 ? 0 : bn + 1;
  }
}

template <int NB4>
__device__ __forceinline__ void store_qk(rsrc_t rs, size_t base, int lane, const f32x4 (&acc)[NB4][2], int a0) {
#pragma unroll
  for (int T = 0; T < 2; ++T)
#pragma unroll
    for (int gr = 0; gr < 4; ++gr) {
      const size_t p = base + (T * 4 + gr) * 1536;
      st16(rs, p + lane * 16, pack8(acc[a0 + 3 * gr][T], acc[a0 + 3 * gr + 1][T]));
      st8(rs, p + 1024 + lane * 8, pack4(acc[a0 + 3 * gr + 2][T]));
    }
}
__device__ __forceinline__ size_t ux_base(int seg, int h, int lane) {
  const int g = lane >> 4, l16 = lane & 15;
  return OFF_UX + ((size_t)(seg * 80 + (l16 >> 3)) * 16 + (h >> 1)) * 1024 + (g * 16 + (h & 1) * 8 + (l16 & 7)) * 16;
}

__device__ __forceinline__ void item_qkvu(unsigned char* ws, rsrc_t rs, unsigned char* smem, int w, int m, int h) {
  const int lane = lane_id();
  f32x4 acc[36][2];
  proj_gemm<36>(ws, smem, w, lane, m, h, 0u, acc);
  RAW_BARRIER();
  u_issue(ws, smem, w, lane, h, 0, 0); u_issue(ws, smem, w, lane, h, 1, 1); u_issue(ws, smem, w, lane, h, 2, 2);
  const int seg = m * 4 + w;
  v4u vf[2][6];
#pragma unroll
  for (int T = 0; T < 2; ++T)
#pragma unroll
    for (int p = 0; p < 6; ++p) vf[T][p] = pack8(acc[24 + 2 * p][T], acc[24 + 2 * p + 1][T]);
  store_qk<36>(rs, OFF_QX + (size_t)(seg * 32 + h) * 12288, lane, acc, 0);
  store_qk<36>(rs, OFF_KX + (size_t)(seg * 32 + h) * 12288, lane, acc, 12);
  u_stage<0, 32>(ws, rs, smem, w, lane, h, vf, 0, 10, ux_base(seg, h, lane));
}
__device__ __forceinline__ void item_piece(unsigned char* ws, rsrc_t rs, unsigned char* smem, int w, int m, int h, int kind) {
  const int lane = lane_id();
  f32x4 acc[12][2];
  proj_gemm<12>(ws, smem, w, lane, m, h, kind == 0 ? (unsigned)OFF_WQF : (kind == 1 ? (unsigned)OFF_WKF : (unsigned)OFF_WVF), acc);
  const int seg = m * 4 + w;
  if (kind < 2) {
    store_qk<12>(rs, (kind ? OFF_KX : OFF_QX) + (size_t)(seg * 32 + h) * 12288, lane, acc, 0);
  } else {
    const int st0 = (kind - 2) * 5;
    RAW_BARRIER();
    u_issue(ws, smem, w, lane, h, st0, 0); u_issue(ws, smem, w, lane, h, st0 + 1, 1); u_issue(ws, smem, w, lane, h, st0 + 2, 2);
    v4u vf[2][6];
#pragma unroll
    for (int T = 0; T < 2; ++T)
#pragma unroll
      for (int p = 0; p < 6; ++p) vf[T][p] = pack8(acc[2 * p][T], acc[2 * p + 1][T]);
    u_stage<0, 0>(ws, rs, smem, w, lane, h, vf, st0, 5, ux_base(seg, h, lane));
  }
}
__device__ __forceinline__ void item_wvo(const u16* wv, unsigned char* ws, rsrc_t rs, unsigned char* smem, int w, int h, int part) {
  const int lane = lane_id(), g = lane >> 4, l16 = lane & 15;
  const int ks = part * 4 + w;
  v4u vf[2][6];
#pragma unroll
  for (int T = 0; T < 2; ++T)
#pragma unroll
    for (int p = 0; p < 6; ++p) {
      const u16* src = wv + (size_t)(ks * 32 + T * 16 + l16) * (NH * DH) + h * DH + 32 * p + 4 * g;
      const v2u lo = *(const v2u*)src, hi = *(const v2u*)(src + 16);
      vf[T][p] = v4u{lo.x, lo.y, hi.x, hi.y};
    }
  wait_v<0>();
  u_issue(ws, smem, w, lane, h, 0, 0); u_issue(ws, smem, w, lane, h, 1, 1); u_issue(ws, smem, w, lane, h, 2, 2);
  const int gp = ((g & 1) << 1) | (g >> 1);
  const size_t ob = OFF_WVOF + ((size_t)((l16 >> 3) * 16 + (h >> 1)) * 20 + ks) * 1024 + (gp * 16 + (h & 1) * 8 + (l16 & 7)) * 16;
  u_stage<1, 0>(ws, rs, smem, w, lane, h, vf, 0, 10, ob);
}

#define FS_DECL do {} while (0)
#define FS(i) do {} while (0)
#define FS_FLUSH() do {} while (0)
#define STAMP(i) do {} while (0)
#define TL(slot) do {} while (0)
__device__ __forceinline__ unsigned long long now() { return __builtin_amdgcn_s_memtime(); }
__device__ __forceinline__ void wait_until(unsigned long long t) { while ((long long)(now() - t) < 0) __builtin_amdgcn_s_sleep(1); }
#define DQ0 4300
#define DCM0 2250
#define DCP0 4100
#define T_DN 22
#define T_UP 96

__device__ __forceinline__ unsigned fetch_mem(__amdgpu_buffer_rsrc_t rs, int s, unsigned char* lmem, int tid) {
  const int base = (int)OFF_MEM + s * 10240;
  const int o0 = base + tid * 16, o1 = base + (tid + 256) * 16, o2 = base + ((tid & 127) + 512) * 16;
  v4u m0, m1, m2;
  unsigned spins = 0;
  for (; spins < SPIN_CAP; ++spins) {
    asm volatile("" ::: "memory");
    m0 = ld_sc1(rs, o0); m1 = ld_sc1(rs, o1); m2 = ld_sc1(rs, o2);
    const bool ok = !(isbad(m0) | isbad(m1) | isbad(m2));
    if (__all(ok)) break;
  }
  *(v4u*)(lmem + tid * 16) = m0;
  *(v4u*)(lmem + (tid + 256) * 16) = m1;
  *(v4u*)(lmem + ((tid & 127) + 512) * 16) = m2;
  return spins;
}

struct ChainT { int dq0, dcm0, dcp0, dn, up, pad; };
__device__ __forceinline__ void chain_role(const u16* wvin, unsigned char* ws, rsrc_t rs, unsigned char* smem, ChainT ct, int w) {
  unsigned char* lmem = smem;
  float* red = (float*)(smem + 10240);
  unsigned char* lw = smem + 10240 + 12288;
  if (blockIdx.x < NH) {
    const int h = blockIdx.x;
    item_piece(ws, rs, smem, w, 0, h, 1);
    signal_done(ws, w, CNT_M);
    const int lane = lane_id(), tid = w * 64 + lane, g = lane >> 4, l16 = lane & 15;
    const int bfo = g * 128 + (l16 & 7) * 16;
    v4u wa[60], wv[40];
    auto wsrc = [&](int ks, int tl) -> const unsigned char* {
      return ws + (tl < 3 ? OFF_WQF : OFF_WKF) + ((size_t)(h * 12 + 3 * w + (tl % 3)) * KS_D + ks) * 1024 + lane * 16;
    };
#pragma unroll
    for (int ks = 0; ks < KS_D; ++ks) {
#pragma unroll
      for (int t = 0; t < 3; ++t) wa[ks * 3 + t] = *(const v4u*)wsrc(ks, t);
#pragma unroll
      for (int t = 0; t < 2; ++t) wv[ks * 2 + t] = *(const v4u*)wsrc(ks, 3 + t);
      *(v4u*)(lw + (w * KS_D + ks) * 1024 + lane * 16) = *(const v4u*)wsrc(ks, 5);
    }
    const unsigned char* lwl = lw + w * KS_D * 1024 + lane * 16;
    const float scale = 0.07216878364870322f * 1.4426950408889634f;
    unsigned long long tref = 0; int dq = ct.dq0;
    for (int s = 0; s < NSEG; ++s) {
      if ((s & 3) == 0) wave_wait_count(ws, CNT_M + (s >> 2), m_target(s >> 2), true);
      const int kx = (int)OFF_KX + (s * 32 + h) * 12288 + w * 1536;
      const v4u kp0 = ld_sc1(rs, kx + lane * 16), kp1 = ld_sc1(rs, kx + 4 * 1536 + lane * 16);
      const v2u ks0 = ld8_sc1(rs, kx + 1024 + lane * 8), ks1 = ld8_sc1(rs, kx + 4 * 1536 + 1024 + lane * 8);
      const int qx = (int)OFF_QX + (s * 32 + h) * 12288 + w * 1536;
      v4u wl[3];
      wl[0] = *(const v4u*)(lwl); wl[1] = *(const v4u*)(lwl + 1024);
      FS_DECL;
      if (s > 0) wait_until(tref + dq);
      FS(0);
      { const unsigned sp = fetch_mem(rs, s, lmem, tid); if (s > 0) dq += sp ? ct.up : -ct.dn;
      }
      FS(1);
      __syncthreads();
      FS(2);
      TL(s);
      if (s > 0 && tid == 64) set_flag(ws, s - 1, h);
      f32x4 acc[6];
#pragma unroll
      for (int t = 0; t < 6; ++t) acc[t] = f32x4{0, 0, 0, 0};
      v4u bq[3];
      bq[0] = *(const v4u*)(lmem + bfo); bq[1] = *(const v4u*)(lmem + 512 + bfo);
#pragma unroll
      for (int ks = 0; ks < KS_D; ++ks) {
        if (ks + 2 < KS_D) {
          bq[(ks + 2) % 3] = *(const v4u*)(lmem + (ks + 2) * 512 + bfo);
          wl[(ks + 2) % 3] = *(const v4u*)(lwl + (ks + 2) * 1024);
        }
        if (ks == 0) asm volatile("s_nop 1" : "+v"(acc[0]), "+v"(acc[1]), "+v"(acc[2]), "+v"(acc[3]), "+v"(acc[4]), "+v"(acc[5]));
        asm volatile(
            "v_mfma_f32_16x16x32_bf16 %0, %6, %12, %0\n\t"
            "v_mfma_f32_16x16x32_bf16 %1, %7, %12, %1\n\t"
            "v_mfma_f32_16x16x32_bf16 %2, %8, %12, %2\n\t"
            "v_mfma_f32_16x16x32_bf16 %3, %9, %12, %3\n\t"
            "v_mfma_f32_16x16x32_bf16 %4, %10, %12, %4\n\t"
            "v_mfma_f32_16x16x32_bf16 %5, %11, %12, %5"
            : "+v"(acc[0]), "+v"(acc[1]), "+v"(acc[2]), "+v"(acc[3]), "+v"(acc[4]), "+v"(acc[5])
            : "a"(wa[ks * 3]), "a"(wa[ks * 3 + 1]), "a"(wa[ks * 3 + 2]), "v"(wv[ks * 2]), "v"(wv[ks * 2 + 1]), "v"(wl[ks % 3]), "v"(bq[ks % 3]));
      }
      asm volatile("s_nop 7\n\ts_nop 1" : "+v"(acc[0]), "+v"(acc[1]), "+v"(acc[2]), "+v"(acc[3]), "+v"(acc[4]), "+v"(acc[5]));
      FS(3);
      const v4u qp = pack8(acc[0], acc[1]);
      const v2u qs2 = pack4(acc[2]);
      const v4u qs = {qs2.x, qs2.y, 0u, 0u};
      const v4u kmp = pack8(acc[3], acc[4]);
      const v2u kms2 = pack4(acc[5]);
      const v4u kms = {kms2.x, kms2.y, 0u, 0u};
      f32x4 z = {0, 0, 0, 0};
      f32x4 s0 = mfma(kmp, qp, z); s0 = mfma(kms, qs, s0);
      f32x4 s1 = mfma(kp0, qp, z); s1 = mfma(v4u{ks0.x, ks0.y, 0u, 0u}, qs, s1);
      f32x4 s2 = mfma(kp1, qp, z); s2 = mfma(v4u{ks1.x, ks1.y, 0u, 0u}, qs, s2);
      if (w != 0) {
        *(f32x4*)(red + ((w * 3 + 0) * 64 + lane) * 4) = s0;
        *(f32x4*)(red + ((w * 3 + 1) * 64 + lane) * 4) = s1;
        *(f32x4*)(red + ((w * 3 + 2) * 64 + lane) * 4) = s2;
      }
      FS(4);
      __syncthreads();
      tref = now();
      FS(5);
      if (w == 0) {
        f32x4 r_[9];
#pragma unroll
        for (int i = 0; i < 9; ++i) r_[i] = *(const f32x4*)(red + ((3 + i) * 64 + lane) * 4);
        __builtin_amdgcn_sched_barrier(0);
        f32x4 t0 = (s0 + r_[0]) + (r_[3] + r_[6]), t1 = (s1 + r_[1]) + (r_[4] + r_[7]), t2 = (s2 + r_[2]) + (r_[5] + r_[8]);
        const bool mv = g < 2;
        if (!mv) t0 = t1;
        float mx = fmaxf(fmaxf(fmaxf(t1.x, t1.y), fmaxf(t1.z, t1.w)), fmaxf(fmaxf(t2.x, t2.y), fmaxf(t2.z, t2.w)));
        mx = fmaxf(mx, fmaxf(fmaxf(t0.x, t0.y), fmaxf(t0.z, t0.w)));
        mx = xmax32(xmax16(mx));
        const float mxs = mx * scale;
        f32x4 e0, e1, e2;
        e0.x = __builtin_amdgcn_exp2f(t0.x * scale - mxs); e0.y = __builtin_amdgcn_exp2f(t0.y * scale - mxs);
        e0.z = __builtin_amdgcn_exp2f(t0.z * scale - mxs); e0.w = __builtin_amdgcn_exp2f(t0.w * scale - mxs);
        e1.x = __builtin_amdgcn_exp2f(t1.x * scale - mxs); e1.y = __builtin_amdgcn_exp2f(t1.y * scale - mxs);
        e1.z = __builtin_amdgcn_exp2f(t1.z * scale - mxs); e1.w = __builtin_amdgcn_exp2f(t1.w * scale - mxs);
        e2.x = __builtin_amdgcn_exp2f(t2.x * scale - mxs); e2.y = __builtin_amdgcn_exp2f(t2.y * scale - mxs);
        e2.z = __builtin_amdgcn_exp2f(t2.z * scale - mxs); e2.w = __builtin_amdgcn_exp2f(t2.w * scale - mxs);
        if (!mv) e0 = z;
        float sum = ((e0.x + e0.y) + (e0.z + e0.w)) + ((e1.x + e1.y) + (e1.z + e1.w)) + ((e2.x + e2.y) + (e2.z + e2.w));
        sum = xadd32(xadd16(sum));
        const float inv = __builtin_amdgcn_rcpf(sum);
        e0 *= inv; e1 *= inv; e2 *= inv;
        v4u px = pack8(e1, e2);
        v2u pm = pack4(e0);
        px.x &= 0x7FFF7FFFu; px.y &= 0x7FFF7FFFu; px.z &= 0x7FFF7FFFu; px.w &= 0x7FFF7FFFu;
        pm.x &= 0x7FFF7FFFu; pm.y &= 0x7FFF7FFFu;
        const int pb = (int)OFF_P + s * 20480 + (h >> 3) * 5120;
        if (l16 < 8) {
          __builtin_amdgcn_raw_buffer_store_b128(px, rs, pb + ((h >> 1) & 3) * 1024 + (g * 16 + (h & 1) * 8 + l16) * 16, 0, 16);
          if (mv) __builtin_amdgcn_raw_buffer_store_b64(pm, rs, pb + 4096 + (((h >> 1) & 3) * 16 + (h & 1) * 8 + l16) * 16 + g * 8, 0, 16);
        }
      }
      FS(6);
      FS_FLUSH();
      __builtin_amdgcn_s_barrier();
      {
        if (s == 0 || s == 4) wave_wait_count(ws, CNT_Q0 + (s >> 2), 32, true);
        const v4u xp0 = ld_sc1(rs, qx + lane * 16), xp1 = ld_sc1(rs, qx + 4 * 1536 + lane * 16);
        const v2u xs0 = ld8_sc1(rs, qx + 1024 + lane * 8), xs1 = ld8_sc1(rs, qx + 4 * 1536 + 1024 + lane * 8);
        const v4u xq0 = {xs0.x, xs0.y, 0u, 0u}, xq1 = {xs1.x, xs1.y, 0u, 0u};
        const v4u k0s = {ks0.x, ks0.y, 0u, 0u}, k1s = {ks1.x, ks1.y, 0u, 0u};
        f32x4* red2 = (f32x4*)(smem + 10240 + 12288 + 81920);
        red2[((w * 2 + 0) * 3 + 0) * 64 + lane] = mfma(kms, xq0, mfma(kmp, xp0, z));
        red2[((w * 2 + 0) * 3 + 1) * 64 + lane] = mfma(k0s, xq0, mfma(kp0, xp0, z));
        red2[((w * 2 + 0) * 3 + 2) * 64 + lane] = mfma(k1s, xq0, mfma(kp1, xp0, z));
        red2[((w * 2 + 1) * 3 + 0) * 64 + lane] = mfma(kms, xq1, mfma(kmp, xp1, z));
        red2[((w * 2 + 1) * 3 + 1) * 64 + lane] = mfma(k0s, xq1, mfma(kp0, xp1, z));
        red2[((w * 2 + 1) * 3 + 2) * 64 + lane] = mfma(k1s, xq1, mfma(kp1, xp1, z));
        __syncthreads();
        if (w >= 2) {
          const int T = w & 1;
          f32x4 t[3];
#pragma unroll
          for (int kt = 0; kt < 3; ++kt)
            t[kt] = (red2[((0 * 2 + T) * 3 + kt) * 64 + lane] + red2[((1 * 2 + T) * 3 + kt) * 64 + lane]) +
                    (red2[((2 * 2 + T) * 3 + kt) * 64 + lane] + red2[((3 * 2 + T) * 3 + kt) * 64 + lane]);
          const int tq = 16 * T + l16;
          const float NEG = -1e30f;
          float v[12];
#pragma unroll
          for (int r = 0; r < 4; ++r) {
            v[r] = g < 2 ? t[0][r] * scale : NEG;
            v[4 + r] = (4 * g + r <= tq) ? t[1][r] * scale : NEG;
            v[8 + r] = (16 + 4 * g + r <= tq) ? t[2][r] * scale : NEG;
          }
          float mx = v[0];
#pragma unroll
          for (int i = 1; i < 12; ++i) mx = fmaxf(mx, v[i]);
          mx = xmax32(xmax16(mx));
          float sum = 0.f;
#pragma unroll
          for (int i = 0; i < 12; ++i) { v[i] = __builtin_amdgcn_exp2f(v[i] - mx); sum += v[i]; }
          sum = xadd32(xadd16(sum));
          const float inv = __builtin_amdgcn_rcpf(sum);
          const size_t pxb = OFF_PX + (size_t)s * 81920;
          st16(rs, pxb + (h * 2 + T) * 1024 + lane * 16,
               v4u{cvtpk(v[4] * inv, v[5] * inv), cvtpk(v[6] * inv, v[7] * inv), cvtpk(v[8] * inv, v[9] * inv), cvtpk(v[10] * inv, v[11] * inv)});
          if (g < 2) st8(rs, pxb + 65536 + ((h >> 2) * 2 + T) * 1024 + ((h & 3) * 16 + l16) * 16 + g * 8,
                         v2u{cvtpk(v[0] * inv, v[1] * inv), cvtpk(v[2] * inv, v[3] * inv)});
        }
      }
    }
    asm volatile("s_waitcnt vmcnt(0)" ::: "memory");
    __syncthreads();
    if (tid == 0) set_flag(ws, NSEG - 1, h);
    TL(64);
  } else if (blockIdx.x < NH + NBK) {
    const int nb = blockIdx.x - NH;
    item_wvo(wvin, ws, rs, smem, w, nb / 5, nb % 5);
    signal_done(ws, w, CNT_WVO);
    TL(1);
    wait_count(ws, w, CNT_WVO, 160);
    TL(2);
    const int lane = lane_id(), tid = w * 64 + lane, g = lane >> 4, l16 = lane & 15;
    const int bfo = g * 128 + (l16 & 7) * 16;
    v4u wa[60], wv[20];
#pragma unroll
    for (int ks = 0; ks < KS_D; ++ks) {
#pragma unroll
      for (int t = 0; t < 3; ++t)
        wa[ks * 3 + t] = *(const v4u*)(ws + OFF_WVOF + ((size_t)(nb * 16 + 4 * w + t) * 20 + ks) * 1024 + lane * 16);
      wv[ks] = *(const v4u*)(ws + OFF_WVOF + ((size_t)(nb * 16 + 4 * w + 3) * 20 + ks) * 1024 + lane * 16);
    }
    asm volatile("s_waitcnt vmcnt(0)" ::: "memory");
    TL(3);
    unsigned char* lu = lw + w * 1024;
    unsigned long long tref = 0, tmem = 0; int dcm = ct.dcm0, dcp = ct.dcp0;
    for (int s = 0; s < NSEG; ++s) {
      if ((s & 3) == 0) wave_wait_count(ws, CNT_M + (s >> 2), m_target(s >> 2), true);
      v4u ux[4];
#pragma unroll
      for (int t = 0; t < 4; ++t) ux[t] = ld_sc1(rs, (int)OFF_UX + ((s * 80 + nb) * 16 + 4 * w + t) * 1024 + lane * 16);
      FS_DECL;
      if (s > 0) wait_until(tref + dcm);
      FS(0);
      { const unsigned sp = fetch_mem(rs, s, lmem, tid); if (s > 0) dcm += sp ? ct.up : -ct.dn; }
      FS(1);
      __syncthreads();
      tmem = now();
      FS(2);
      f32x4 acc[4];
#pragma unroll
      for (int t = 0; t < 4; ++t) acc[t] = f32x4{0, 0, 0, 0};
      v4u aq[3];
      aq[0] = *(const v4u*)(lmem + bfo); aq[1] = *(const v4u*)(lmem + 512 + bfo);
#pragma unroll
      for (int ks = 0; ks < KS_D; ++ks) {
        if (ks + 2 < KS_D) aq[(ks + 2) % 3] = *(const v4u*)(lmem + (ks + 2) * 512 + bfo);
        if (ks == 0) asm volatile("s_nop 1" : "+v"(acc[0]), "+v"(acc[1]), "+v"(acc[2]), "+v"(acc[3]));
        asm volatile(
            "v_mfma_f32_16x16x32_bf16 %0, %8, %4, %0\n\t"
            "v_mfma_f32_16x16x32_bf16 %1, %8, %5, %1\n\t"
            "v_mfma_f32_16x16x32_bf16 %2, %8, %6, %2\n\t"
            "v_mfma_f32_16x16x32_bf16 %3, %8, %7, %3"
            : "+v"(acc[0]), "+v"(acc[1]), "+v"(acc[2]), "+v"(acc[3])
            : "a"(wa[ks * 3]), "a"(wa[ks * 3 + 1]), "a"(wa[ks * 3 + 2]), "v"(wv[ks]), "v"(aq[ks % 3]));
      }
      asm volatile("s_nop 7\n\ts_nop 1" : "+v"(acc[0]), "+v"(acc[1]), "+v"(acc[2]), "+v"(acc[3]));
      if (g < 2) {
#pragma unroll
        for (int t = 0; t < 4; ++t) *(v2u*)(lu + (t * 16 + l16) * 16 + g * 8) = pack4(acc[t]);
      }
      asm volatile("s_waitcnt lgkmcnt(0)" ::: "memory");
      const v4u um = *(const v4u*)(lu + (g * 16 + l16) * 16);
      st16(rs, OFF_UM + ((size_t)(s * 32 + 8 * w + 2 * g + (l16 >> 3)) * 640 + 8 * nb + (l16 & 7)) * 16, um);
      const int pb = (int)OFF_P + s * 20480 + w * 5120 + lane * 16;
      v4u p0, p1, p2, p3, p4;
      FS(3);
      wait_until(tmem + dcp);
      FS(4);
      unsigned spins = 0;
      for (; spins < SPIN_CAP; ++spins) {
        asm volatile("" ::: "memory");
        p0 = ld_sc1(rs, pb); p1 = ld_sc1(rs, pb + 1024); p2 = ld_sc1(rs, pb + 2048); p3 = ld_sc1(rs, pb + 3072); p4 = ld_sc1(rs, pb + 4096);
        const bool ok = !(isbad(p0) | isbad(p1) | isbad(p2) | isbad(p3) | isbad(p4));
        if (__all(ok)) break;
      }
      dcp += spins ? ct.up : -ct.dn;
      FS(5);
      f32x4 d = {0, 0, 0, 0}, d2 = {0, 0, 0, 0};
      d = mfma(ux[0], p0, d); d2 = mfma(ux[1], p1, d2); d = mfma(ux[2], p2, d); d2 = mfma(ux[3], p3, d2); d = mfma(um, p4, d);
      d += d2;
      if ((l16 < 8) == (g < 2)) *(f32x4*)(red + (((w * 2 + (g >> 1)) * 8 + (l16 & 7)) * 8 + (g & 1) * 4)) = d;
      FS(6);
      __syncthreads();
      tref = now();
      FS(7);
      if (tid == 64) set_flag(ws, s, 32 + nb);
      if (w == 0) {
        float r_[8];
#pragma unroll
        for (int q = 0; q < 8; ++q) r_[q] = red[q * 64 + lane];
        __builtin_amdgcn_sched_barrier(0);
        const float v = ((r_[0] + r_[1]) + (r_[2] + r_[3])) + ((r_[4] + r_[5]) + (r_[6] + r_[7]));
        const float vn = __builtin_bit_cast(float, __builtin_amdgcn_mov_dpp(__builtin_bit_cast(int, v), 0xB1, 0xF, 0xF, true));
        const unsigned d0 = desent(cvtpk(v, vn));
        const unsigned d1 = (unsigned)__builtin_amdgcn_mov_dpp((int)d0, 0x4E, 0xF, 0xF, true);
        const unsigned e0 = (unsigned)__builtin_amdgcn_mov_dpp((int)d0, 0x104, 0xF, 0xF, true);
        const unsigned e1 = (unsigned)__builtin_amdgcn_mov_dpp((int)d1, 0x104, 0xF, 0xF, true);
        if ((lane & 7) == 0)
          __builtin_amdgcn_raw_buffer_store_b128(v4u{d0, d1, e0, e1}, rs, (int)OFF_MEM + (s + 1) * 10240 + nb * 128 + (lane >> 3) * 16, 0, 16);
      }
      FS(8);
      FS_FLUSH();
    }
  }
}

__device__ __forceinline__ void item_pb(const u16* x, unsigned char* ws, rsrc_t rs, unsigned char* smem, int w, int seg, int part) {
  const int lane = lane_id(), g = lane >> 4, l16 = lane & 15;
  const int pxb = (int)OFF_PX + seg * 81920 + lane * 16;
  v4u pf[10][2], uf[10][4];
  wave_wait_count(ws, CNT_M + (seg >> 2), m_target(seg >> 2));
#pragma unroll
  for (int i = 0; i < 8; ++i) {
    const int h = 8 * w + i;
#pragma unroll
    for (int c = 0; c < 4; ++c)
      uf[i][c] = ld_sc1(rs, (int)OFF_UX + ((seg * 80 + 2 * (part * 4 + c) + (l16 >> 3)) * 16 + (h >> 1)) * 1024 + (g * 16 + (h & 1) * 8 + (l16 & 7)) * 16);
  }
  const int pr = w >> 1, Tf = w & 1;
  v2u xv[2];
#pragma unroll
  for (int i = 0; i < 2; ++i) xv[i] = *(const v2u*)(x + (size_t)(seg * 32 + 16 * Tf + l16) * DM + 16 * (part * 4 + 2 * pr + i) + 4 * g);
  __builtin_amdgcn_sched_barrier(0);
  wave_wait_flags(ws, seg, 8 * w, 8, lane, seg >= FAST_SEG);
  wave_wait_flags(ws, seg, 32 + 8 * part, 8, lane, seg >= FAST_SEG);
  if (seg == 63) TL(81);
#pragma unroll
  for (int i = 0; i < 8; ++i)
#pragma unroll
    for (int T = 0; T < 2; ++T) pf[i][T] = ld_sc1(rs, pxb + ((8 * w + i) * 2 + T) * 1024);
#pragma unroll
  for (int i = 0; i < 2; ++i) {
    const int hq = 2 * w + i;
#pragma unroll
    for (int T = 0; T < 2; ++T) pf[8 + i][T] = ld_sc1(rs, pxb + 65536 + (hq * 2 + T) * 1024);
#pragma unroll
    for (int c = 0; c < 4; ++c)
      uf[8 + i][c] = ld_sc1(rs, (int)OFF_UM + ((seg * 32 + 4 * hq + g) * 640 + 16 * (part * 4 + c) + l16) * 16);
  }
  __builtin_amdgcn_sched_barrier(0);
  f32x4 acc[4][2];
#pragma unroll
  for (int c = 0; c < 4; ++c) { acc[c][0] = f32x4{0, 0, 0, 0}; acc[c][1] = f32x4{0, 0, 0, 0}; }
#pragma unroll
  for (int i = 0; i < 10; ++i)
#pragma unroll
    for (int c = 0; c < 4; ++c) {
      acc[c][0] = mfma(uf[i][c], pf[i][0], acc[c][0]);
      acc[c][1] = mfma(uf[i][c], pf[i][1], acc[c][1]);
    }
  f32x4* red = (f32x4*)smem;
#pragma unroll
  for (int c = 0; c < 4; ++c)
#pragma unroll
    for (int T = 0; T < 2; ++T) red[((w * 4 + c) * 2 + T) * 64 + lane] = acc[c][T];
  __syncthreads();
  {
    f32x4 o[2];
#pragma unroll
    for (int i = 0; i < 2; ++i) {
      const int c = 2 * pr + i;
      o[i] = (red[((0 * 4 + c) * 2 + Tf) * 64 + lane] + red[((1 * 4 + c) * 2 + Tf) * 64 + lane]) +
             (red[((2 * 4 + c) * 2 + Tf) * 64 + lane] + red[((3 * 4 + c) * 2 + Tf) * 64 + lane]);
      o[i].x = bflo(xv[i].x) + rbf(o[i].x); o[i].y = bfhi(xv[i].x) + rbf(o[i].y);
      o[i].z = bflo(xv[i].y) + rbf(o[i].z); o[i].w = bfhi(xv[i].y) + rbf(o[i].w);
    }
    st16(rs, OFF_HF + ((size_t)(seg * 2 + Tf) * KS_D + part * 2 + pr) * 1024 + lane * 16, pack8(o[0], o[1]));
  }
}

__device__ __forceinline__ float gelu_tanh(float x) {
  const float u = 0.7978845608028654f * (x + 0.044715f * x * x * x);
  const float e = __expf(2.f * u);
  const float t = 1.f - 2.f / (1.f + e);
  return 0.5f * x * (1.f + t);
}

__device__ __forceinline__ void item_f1(unsigned char* ws, rsrc_t rs, unsigned char* smem, int w, int seg, int part) {
  const int lane = lane_id(), g = lane >> 4, l16 = lane & 15;
  const unsigned char* wg = ws + OFF_WGF + (size_t)(part * 8 + 2 * w) * KS_D * 1024 + lane * 16;
  const unsigned char* wu = ws + OFF_WUF + (size_t)(part * 8 + 2 * w) * KS_D * 1024 + lane * 16;
  v4u wf[KS_D][4];
#pragma unroll
  for (int ks = 0; ks < KS_D; ++ks) {
    wf[ks][0] = *(const v4u*)(wg + ks * 1024); wf[ks][1] = *(const v4u*)(wg + (KS_D + ks) * 1024);
    wf[ks][2] = *(const v4u*)(wu + ks * 1024); wf[ks][3] = *(const v4u*)(wu + (KS_D + ks) * 1024);
  }
  __builtin_amdgcn_sched_barrier(0);
  wave_wait_count(ws, CNT_PB + seg, 10, seg >= FAST_SEG);
  if (seg == 63) TL(82);
  v4u hv[2][5];
#pragma unroll
  for (int T = 0; T < 2; ++T)
#pragma unroll
    for (int i = 0; i < 5; ++i) hv[T][i] = ld_sc1(rs, (int)OFF_HF + ((seg * 2 + T) * KS_D + 5 * w + i) * 1024 + lane * 16);
  __builtin_amdgcn_sched_barrier(0);
  float* lred = (float*)(smem + 40960);
  float mean[2], rstd[2];
#pragma unroll
  for (int T = 0; T < 2; ++T) {
    float sacc = 0.f;
#pragma unroll
    for (int i = 0; i < 5; ++i) {
      const v4u u = hv[T][i];
      sacc += (bflo(u.x) + bfhi(u.x)) + (bflo(u.y) + bfhi(u.y)) + (bflo(u.z) + bfhi(u.z)) + (bflo(u.w) + bfhi(u.w));
    }
    sacc = xadd32(xadd16(sacc));
    if (g == 0) lred[w * 32 + 16 * T + l16] = sacc;
  }
  __syncthreads();
#pragma unroll
  for (int T = 0; T < 2; ++T) {
    mean[T] = ((lred[16 * T + l16] + lred[32 + 16 * T + l16]) + (lred[64 + 16 * T + l16] + lred[96 + 16 * T + l16])) * (1.f / DM);
    float q = 0.f;
#pragma unroll
    for (int i = 0; i < 5; ++i) {
      const unsigned u[4] = {hv[T][i].x, hv[T][i].y, hv[T][i].z, hv[T][i].w};
#pragma unroll
      for (int j = 0; j < 4; ++j) { const float d0 = bflo(u[j]) - mean[T], d1 = bfhi(u[j]) - mean[T]; q += d0 * d0 + d1 * d1; }
    }
    q = xadd32(xadd16(q));
    if (g == 0) lred[128 + w * 32 + 16 * T + l16] = q;
  }
  __syncthreads();
#pragma unroll
  for (int T = 0; T < 2; ++T) {
    const float vv = ((lred[128 + 16 * T + l16] + lred[160 + 16 * T + l16]) + (lred[192 + 16 * T + l16] + lred[224 + 16 * T + l16])) * (1.f / DM);
    rstd[T] = rsqrtf(vv + 1e-5f);
#pragma unroll
    for (int i = 0; i < 5; ++i) {
      const unsigned u[4] = {hv[T][i].x, hv[T][i].y, hv[T][i].z, hv[T][i].w};
      v4u o;
      o.x = cvtpk((bflo(u[0]) - mean[T]) * rstd[T], (bfhi(u[0]) - mean[T]) * rstd[T]);
      o.y = cvtpk((bflo(u[1]) - mean[T]) * rstd[T], (bfhi(u[1]) - mean[T]) * rstd[T]);
      o.z = cvtpk((bflo(u[2]) - mean[T]) * rstd[T], (bfhi(u[2]) - mean[T]) * rstd[T]);
      o.w = cvtpk((bflo(u[3]) - mean[T]) * rstd[T], (bfhi(u[3]) - mean[T]) * rstd[T]);
      *(v4u*)(smem + (T * KS_D + 5 * w + i) * 1024 + lane * 16) = o;
    }
  }
  __syncthreads();
  f32x4 acc[4][2];
#pragma unroll
  for (int a = 0; a < 4; ++a) { acc[a][0] = f32x4{0, 0, 0, 0}; acc[a][1] = f32x4{0, 0, 0, 0}; }
#pragma unroll
  for (int ks = 0; ks < KS_D; ++ks) {
    const v4u b0 = *(const v4u*)(smem + ks * 1024 + lane * 16), b1 = *(const v4u*)(smem + (KS_D + ks) * 1024 + lane * 16);
#pragma unroll
    for (int a = 0; a < 4; ++a) { acc[a][0] = mfma(wf[ks][a], b0, acc[a][0]); acc[a][1] = mfma(wf[ks][a], b1, acc[a][1]); }
  }
#pragma unroll
  for (int T = 0; T < 2; ++T) {
    f32x4 o[2];
#pragma unroll
    for (int i = 0; i < 2; ++i)
#pragma unroll
      for (int r = 0; r < 4; ++r) {
        const float gg = rbf(acc[i][T][r]), uu = rbf(acc[2 + i][T][r]);
        o[i][r] = rbf(gelu_tanh(gg)) * uu;
      }
    st16(rs, OFF_ACTF + ((size_t)(seg * 2 + T) * KS_F + part * 4 + w) * 1024 + lane * 16, pack8(o[0], o[1]));
  }
}

__device__ __forceinline__ void item_f2(unsigned char* ws, rsrc_t rs, unsigned char* smem, u16* out, int w, int seg, int part) {
  const int lane = lane_id(), g = lane >> 4, l16 = lane & 15;
  v4u af[12][4], bf[12][2];
#pragma unroll
  for (int i = 0; i < 12; ++i)
#pragma unroll
    for (int c = 0; c < 4; ++c) af[i][c] = *(const v4u*)(ws + OFF_WDF + ((size_t)(part * 4 + c) * KS_F + 12 * w + i) * 1024 + lane * 16);
  __builtin_amdgcn_sched_barrier(0);
  wave_wait_count(ws, CNT_F1 + seg, 12, seg >= FAST_SEG);
  if (seg == 63) TL(83);
#pragma unroll
  for (int i = 0; i < 12; ++i)
#pragma unroll
    for (int T = 0; T < 2; ++T) bf[i][T] = ld_sc1(rs, (int)OFF_ACTF + ((seg * 2 + T) * KS_F + 12 * w + i) * 1024 + lane * 16);
  const int pr = w >> 1, Tf = w & 1;
  const v4u hres = ld_sc1(rs, (int)OFF_HF + ((seg * 2 + Tf) * KS_D + part * 2 + pr) * 1024 + lane * 16);
  __builtin_amdgcn_sched_barrier(0);
  f32x4 acc[4][2];
#pragma unroll
  for (int c = 0; c < 4; ++c) { acc[c][0] = f32x4{0, 0, 0, 0}; acc[c][1] = f32x4{0, 0, 0, 0}; }
#pragma unroll
  for (int i = 0; i < 12; ++i)
#pragma unroll
    for (int c = 0; c < 4; ++c) { acc[c][0] = mfma(af[i][c], bf[i][0], acc[c][0]); acc[c][1] = mfma(af[i][c], bf[i][1], acc[c][1]); }
  f32x4* red = (f32x4*)smem;
#pragma unroll
  for (int c = 0; c < 4; ++c)
#pragma unroll
    for (int T = 0; T < 2; ++T) red[((w * 4 + c) * 2 + T) * 64 + lane] = acc[c][T];
  __syncthreads();
  {
    const unsigned hr[4] = {hres.x, hres.y, hres.z, hres.w};
    const int tok = seg * 32 + 16 * Tf + l16;
#pragma unroll
    for (int i = 0; i < 2; ++i) {
      const int c = 2 * pr + i;
      f32x4 o = (red[((0 * 4 + c) * 2 + Tf) * 64 + lane] + red[((1 * 4 + c) * 2 + Tf) * 64 + lane]) +
                (red[((2 * 4 + c) * 2 + Tf) * 64 + lane] + red[((3 * 4 + c) * 2 + Tf) * 64 + lane]);
      o.x = bflo(hr[2 * i]) + rbf(o.x); o.y = bfhi(hr[2 * i]) + rbf(o.y);
      o.z = bflo(hr[2 * i + 1]) + rbf(o.z); o.w = bfhi(hr[2 * i + 1]) + rbf(o.w);
      *(v2u*)(out + (size_t)tok * DM + 16 * (part * 4 + c) + 4 * g) = pack4(o);
    }
  }
  __syncthreads();
}

constexpr int NCHAIN = NH + NBK, NWORK = 256 - NCHAIN;
constexpr int IT_QKR = 448, IT_PRE = 160 + IT_QKR, IT_ROUND = 32, N_ROUNDS = NSEG + 2;
constexpr int IT_TOTAL = IT_PRE + N_ROUNDS * IT_ROUND;
struct MainArgs { const u16* x; const u16* wv; unsigned char* ws; u16* out; ChainT ct; };

__global__ void __launch_bounds__(256, 1) k_main(MainArgs a) {
  extern __shared__ __attribute__((aligned(16))) unsigned char smem[];
  unsigned char* ws = a.ws;
  const rsrc_t rs = __builtin_amdgcn_make_buffer_rsrc((void*)ws, 0, (int)WS_TOTAL, 0x00020000);
  TL(100);
  const int w = __builtin_amdgcn_readfirstlane(threadIdx.x >> 6);
  if (blockIdx.x < NCHAIN) { chain_role(a.wv, ws, rs, smem, a.ct, w); return; }
  const int wk = blockIdx.x - NCHAIN;
  if (wk < 80) {
    item_wvo(a.wv, ws, rs, smem, w, (80 + wk) / 5, (80 + wk) % 5);
    signal_done(ws, w, CNT_WVO);
  } else {
    item_piece(ws, rs, smem, w, 0, (wk - 80) & 31, 2 + ((wk - 80) >> 5));
    signal_done(ws, w, CNT_M);
  }
  TL(110);
  int it = wk;
  for (; it < IT_PRE; it += NWORK) {
    if (it < 32) {
      item_piece(ws, rs, smem, w, 0, it, 0);
      signal_done(ws, w, CNT_Q0);
    } else if (it < 160) {
      const int j = it - 32;
      item_piece(ws, rs, smem, w, 1, j & 31, j < 64 ? 2 + (j >> 5) : (j < 96 ? 1 : 0));
      signal_done(ws, w, j < 96 ? CNT_M + 1 : CNT_Q0 + 1);
    } else {
      const int j = it - 160 + 64;
      item_qkvu(ws, rs, smem, w, j >> 5, j & 31);
      signal_done(ws, w, CNT_M + (j >> 5));
    }
    TL(111 + it / NWORK);
  }
  TL(101);
  for (; it < IT_TOTAL; it += NWORK) {
    const int j = it - IT_PRE, r = j / IT_ROUND, q = j % IT_ROUND;
    TL(j / NWORK);
    if (q < 10) {
      const int seg = r;
      if (seg >= NSEG) continue;
      item_pb(a.x, ws, rs, smem, w, seg, q);
      if (seg == 63) TL(72);
      signal_done(ws, w, CNT_PB + seg);
      if (seg == 63) TL(73);
    } else if (q < 22) {
      const int seg = r - 1;
      if (seg < 0 || seg >= NSEG) continue;
      item_f1(ws, rs, smem, w, seg, q - 10);
      if (seg == 63) TL(74);
      signal_done(ws, w, CNT_F1 + seg);
      if (seg == 63) TL(75);
    } else {
      const int seg = r - 2;
      if (seg < 0 || seg >= NSEG) continue;
      item_f2(ws, rs, smem, a.out, w, seg, q - 22);
      if (seg == 63) TL(76);
    }
  }
  TL(102);
}

extern "C" void kernel_launch(void* const* d_in, const int* in_sizes, int n_in, void* d_out, int out_size, void* d_ws,
                              size_t ws_size, hipStream_t stream) {
  unsigned char* ws = (unsigned char*)d_ws;
  PrepArgs pa;
  pa.x = (const u16*)d_in[0]; pa.mem0 = (const u16*)d_in[1]; pa.wq = (const u16*)d_in[2]; pa.wk = (const u16*)d_in[3];
  pa.wv = (const u16*)d_in[4]; pa.wo = (const u16*)d_in[5]; pa.wg = (const u16*)d_in[8]; pa.wu = (const u16*)d_in[9];
  pa.wd = (const u16*)d_in[10]; pa.ws = ws;
  constexpr int LDS_MAIN = 136 * 1024;
  (void)hipFuncSetAttribute((const void*)k_main, hipFuncAttributeMaxDynamicSharedMemorySize, LDS_MAIN);
  k_prep<<<PREP_BLOCKS, 256, 0, stream>>>(pa);
  MainArgs ma;
  ma.x = pa.x; ma.wv = pa.wv; ma.ws = ws; ma.out = (u16*)d_out;
  ma.ct.dq0 = DQ0; ma.ct.dcm0 = DCM0; ma.ct.dcp0 = DCP0; ma.ct.dn = T_DN; ma.ct.up = T_UP; ma.ct.pad = 0;
  k_main<<<256, 256, LDS_MAIN, stream>>>(ma);
}
```

```cpp
#include <hip/hip_runtime.h>
#include <hip/hip_bf16.h>
#include <stdint.h>

constexpr int SEQ = 2048, DM = 640, NH = 32, DH = 192, NM = 8, SEG = 32, NSEG = 64, DF = 1536;
constexpr int KS_D = DM / 32;
constexpr int KS_F = DF / 32;
constexpr int NBK = 80;

constexpr size_t OFF_XNF  = 0;
constexpr size_t OFF_WQF  = OFF_XNF  + (size_t)128 * 20 * 1024;
constexpr size_t OFF_WKF  = OFF_WQF  + (size_t)384 * 20 * 1024;
constexpr size_t OFF_WVF  = OFF_WKF  + (size_t)384 * 20 * 1024;
constexpr size_t OFF_WOF  = OFF_WVF  + (size_t)384 * 20 * 1024;
constexpr size_t OFF_WGF  = OFF_WOF  + (size_t)32 * 40 * 6 * 1024;
constexpr size_t OFF_WUF  = OFF_WGF  + (size_t)96 * 20 * 1024;
constexpr size_t OFF_WDF  = OFF_WUF  + (size_t)96 * 20 * 1024;
constexpr size_t OFF_WVOF = OFF_WDF  + (size_t)40 * 48 * 1024;
constexpr size_t OFF_QX   = OFF_WVOF + (size_t)80 * 16 * 20 * 1024;
constexpr size_t OFF_KX   = OFF_QX   + (size_t)64 * 32 * 12288;
constexpr size_t OFF_UX   = OFF_KX   + (size_t)64 * 32 * 12288;
constexpr size_t OFF_MEM  = OFF_UX   + (size_t)64 * 80 * 16 * 1024;
constexpr size_t OFF_P    = OFF_MEM  + (size_t)65 * 10240;
constexpr size_t OFF_KM   = OFF_P    + (size_t)64 * 20480;
constexpr size_t OFF_UM   = OFF_KM   + (size_t)64 * 32 * 6144;
constexpr size_t OFF_PX   = OFF_UM   + (size_t)64 * 32 * 10240;
constexpr size_t OFF_HF   = OFF_PX   + (size_t)64 * 81920;
constexpr size_t OFF_ACTF = OFF_HF   + (size_t)128 * 20 * 1024;
constexpr size_t OFF_CNT  = OFF_ACTF + (size_t)128 * 48 * 1024;
constexpr int CNT_WVO = 0, CNT_M = 1, CNT_SEG = 17, CNT_PA = 81, CNT_PB = 145, CNT_F1 = 209, CNT_Q0 = 273, CNT_N = 275;
__host__ __device__ constexpr unsigned m_target(int m) { return m <= 1 ? 96u : 32u; }
constexpr size_t OFF_FLG  = OFF_CNT + (size_t)CNT_N * 256;
constexpr size_t CTRL_BYTES = (size_t)CNT_N * 256 + 64 * 512;
constexpr size_t WS_TOTAL = OFF_CNT + CTRL_BYTES + 256;
static_assert(WS_TOTAL < (size_t)256 * 1024 * 1024, "ws too big");

typedef short bf16x8 __attribute__((ext_vector_type(8)));
typedef float f32x4 __attribute__((ext_vector_type(4)));
typedef unsigned v4u __attribute__((ext_vector_type(4)));
typedef unsigned v2u __attribute__((ext_vector_type(2)));
typedef unsigned short u16;

__device__ __forceinline__ f32x4 mfma(v4u a, v4u b, f32x4 c) {
  return __builtin_amdgcn_mfma_f32_16x16x32_bf16(__builtin_bit_cast(bf16x8, a), __builtin_bit_cast(bf16x8, b), c, 0, 0, 0);
}
typedef __bf16 bf16x2_t __attribute__((ext_vector_type(2)));
typedef float f32x2 __attribute__((ext_vector_type(2)));
__device__ __forceinline__ unsigned cvtpk(float lo, float hi) {
  f32x2 v = {lo, hi};
  return __builtin_bit_cast(unsigned, __builtin_convertvector(v, bf16x2_t));
}
__device__ __forceinline__ float bf2f(unsigned short b) { return __builtin_bit_cast(float, (unsigned)b << 16); }
__device__ __forceinline__ float bflo(unsigned u) { return __builtin_bit_cast(float, u << 16); }
__device__ __forceinline__ float bfhi(unsigned u) { return __builtin_bit_cast(float, u & 0xffff0000u); }
__device__ __forceinline__ float rbf(float x) { return bflo(cvtpk(x, x)); }
__device__ __forceinline__ v4u pack8(f32x4 a, f32x4 b) {
  v4u r; r.x = cvtpk(a.x, a.y); r.y = cvtpk(a.z, a.w); r.z = cvtpk(b.x, b.y); r.w = cvtpk(b.z, b.w); return r;
}
__device__ __forceinline__ v2u pack4(f32x4 a) { v2u r; r.x = cvtpk(a.x, a.y); r.y = cvtpk(a.z, a.w); return r; }
constexpr unsigned SENT = 0xFFFFFFFFu;
__device__ __forceinline__ unsigned desent(unsigned x) { return x == SENT ? 0x7FC07FC0u : x; }
__device__ __forceinline__ bool isbad(v4u v) { return (v.x == SENT) | (v.y == SENT) | (v.z == SENT) | (v.w == SENT); }

#define RAW_BARRIER() do { asm volatile("s_waitcnt lgkmcnt(0)" ::: "memory"); __builtin_amdgcn_s_barrier(); } while (0)
#define WAIT_V(n) asm volatile("s_waitcnt vmcnt(" #n ")" ::: "memory")

__device__ __forceinline__ void glds16(const unsigned char* g, unsigned char* l) {
  __builtin_amdgcn_global_load_lds((const unsigned*)g, (unsigned*)l, 16, 0, 0);
}

typedef __amdgpu_buffer_rsrc_t rsrc_t;
__device__ __forceinline__ int lane_id() {
  int l; asm volatile("v_mbcnt_lo_u32_b32 %0, -1, 0\n\tv_mbcnt_hi_u32_b32 %0, -1, %0" : "=v"(l)); return l;
}
__device__ __forceinline__ void st16(rsrc_t rs, size_t off, v4u v) { __builtin_amdgcn_raw_buffer_store_b128(v, rs, (int)off, 0, 16); }
__device__ __forceinline__ void st8(rsrc_t rs, size_t off, v2u v) { __builtin_amdgcn_raw_buffer_store_b64(v, rs, (int)off, 0, 16); }
__device__ __forceinline__ unsigned* cnt_ptr(unsigned char* ws, int i) { return (unsigned*)(ws + OFF_CNT + (size_t)i * 256); }
constexpr unsigned SPIN_CAP = 1u << 19;
#define POLL_FAST 4
#define FAST_SEG 63
#define POLL_SLEEP 64
__device__ __forceinline__ void signal_done(unsigned char* ws, int w, int i) {
  asm volatile("s_waitcnt vmcnt(0)" ::: "memory");
  __syncthreads();
  if (w == 0 && lane_id() == 0) __hip_atomic_fetch_add(cnt_ptr(ws, i), 1u, __ATOMIC_RELAXED, __HIP_MEMORY_SCOPE_AGENT);
}
__device__ __forceinline__ void wait_count(unsigned char* ws, int w, int i, unsigned target) {
  if (w == 0 && lane_id() == 0) {
    unsigned* p = cnt_ptr(ws, i);
    for (unsigned spins = 0; spins < SPIN_CAP; ++spins) {
      if (__hip_atomic_load(p, __ATOMIC_RELAXED, __HIP_MEMORY_SCOPE_AGENT) >= target) break;
      __builtin_amdgcn_s_sleep(2);
    }
    __builtin_amdgcn_fence(__ATOMIC_ACQUIRE, "agent");
    asm volatile("s_waitcnt vmcnt(0)" ::: "memory");
  }
  __syncthreads();
}
__device__ __forceinline__ void wave_wait_count(unsigned char* ws, int i, unsigned target, bool fast = false) {
  unsigned* p = cnt_ptr(ws, i);
  for (unsigned spins = 0; spins < SPIN_CAP; ++spins) {
    if (__builtin_amdgcn_readfirstlane(__hip_atomic_load(p, __ATOMIC_RELAXED, __HIP_MEMORY_SCOPE_AGENT)) >= target) break;
    if (fast) __builtin_amdgcn_s_sleep(POLL_FAST); else __builtin_amdgcn_s_sleep(POLL_SLEEP);
  }
  __builtin_amdgcn_fence(__ATOMIC_ACQUIRE, "wavefront");
}
__device__ __forceinline__ void set_flag(unsigned char* ws, int seg, int i) {
  __hip_atomic_store((unsigned*)(ws + OFF_FLG + (size_t)seg * 512) + i, 1u, __ATOMIC_RELAXED, __HIP_MEMORY_SCOPE_AGENT);
}
__device__ __forceinline__ void wave_wait_flags(unsigned char* ws, int seg, int i0, int n, int lane, bool fast = false) {
  unsigned* p = (unsigned*)(ws + OFF_FLG + (size_t)seg * 512) + i0 + (lane < n ? lane : 0);
  for (unsigned spins = 0; spins < SPIN_CAP; ++spins) {
    if (__all(__hip_atomic_load(p, __ATOMIC_RELAXED, __HIP_MEMORY_SCOPE_AGENT) != 0u)) break;
    if (fast) __builtin_amdgcn_s_sleep(POLL_FAST); else __builtin_amdgcn_s_sleep(POLL_SLEEP);
  }
  __builtin_amdgcn_fence(__ATOMIC_ACQUIRE, "wavefront");
}

__device__ __forceinline__ void swap16(float v, float& a, float& b) {
  a = v; b = v;
  asm volatile("s_nop 1\n\tv_permlane16_swap_b32 %0, %1\n\ts_nop 1" : "+v"(a), "+v"(b));
}
__device__ __forceinline__ void swap32(float v, float& a, float& b) {
  a = v; b = v;
  asm volatile("s_nop 1\n\tv_permlane32_swap_b32 %0, %1\n\ts_nop 1" : "+v"(a), "+v"(b));
}
__device__ __forceinline__ float xmax16(float v) { float a, b; swap16(v, a, b); return fmaxf(a, b); }
__device__ __forceinline__ float xmax32(float v) { float a, b; swap32(v, a, b); return fmaxf(a, b); }
__device__ __forceinline__ float xadd16(float v) { float a, b; swap16(v, a, b); return a + b; }
__device__ __forceinline__ float xadd32(float v) { float a, b; swap32(v, a, b); return a + b; }
__device__ __forceinline__ v4u ld_sc1(__amdgpu_buffer_rsrc_t rs, int off) { return __builtin_amdgcn_raw_buffer_load_b128(rs, off, 0, 16); }
__device__ __forceinline__ v2u ld8_sc1(__amdgpu_buffer_rsrc_t rs, int off) { return __builtin_amdgcn_raw_buffer_load_b64(rs, off, 0, 16); }
__device__ __forceinline__ void swapu16(unsigned& a, unsigned& b) { asm volatile("s_nop 1\n\tv_permlane16_swap_b32 %0, %1\n\ts_nop 1" : "+v"(a), "+v"(b)); }

struct PrepArgs {
  const u16* x; const u16* mem0; const u16* wq; const u16* wk; const u16* wv; const u16* wo;
  const u16* wg; const u16* wu; const u16* wd; unsigned char* ws;
};

__device__ __forceinline__ void relayout_tile(const u16* src, int ld, int ks, int n0, int ncols, unsigned char* dst, size_t frag0, size_t frag_stride,
                                              bool perm, unsigned char* tile) {
  constexpr int RS = 528;
  const int t = threadIdx.x;
#pragma unroll
  for (int i = 0; i < 4; ++i) {
    const int id = t + 256 * i, row = id >> 5, c8 = id & 31;
    if (c8 * 8 < ncols) *(v4u*)(tile + row * RS + c8 * 16) = *(const v4u*)(src + (size_t)(ks * 32 + row) * ld + n0 + c8 * 8);
  }
  __syncthreads();
  const int lane = t & 63, w = t >> 6, g = lane >> 4, q = (lane >> 2) & 3, p = lane & 3;
  const int r0 = (perm ? 4 * g : 8 * g) + q, r1 = (perm ? 16 + 4 * g : 8 * g + 4) + q;
  const unsigned base = (unsigned)(size_t)tile;
#pragma unroll
  for (int i = 0; i < 4; ++i) {
    const int f = w * 4 + i;
    if (f * 16 < ncols) {
      v2u lo, hi;
      asm volatile("ds_read_b64_tr_b16 %0, %2\n\tds_read_b64_tr_b16 %1, %3\n\ts_waitcnt lgkmcnt(0)"
                   : "=&v"(lo), "=&v"(hi) : "v"(base + r0 * RS + f * 32 + p * 8), "v"(base + r1 * RS + f * 32 + p * 8) : "memory");
      *(v4u*)(dst + (frag0 + (size_t)f * frag_stride) * 1024 + lane * 16) = v4u{lo.x, lo.y, hi.x, hi.y};
    }
  }
}

constexpr int PREP_LN = 128, PREP_WQKV = 20 * 24, PREP_WO = 192 * 3, PREP_WGU = 20 * 6, PREP_WD = 48 * 3, PREP_FILL = 64;
constexpr int PREP_BLOCKS = PREP_LN + 3 * PREP_WQKV + PREP_WO + 2 * PREP_WGU + PREP_WD + PREP_FILL;

__global__ void __launch_bounds__(256) k_prep(PrepArgs a) {
  __shared__ __attribute__((aligned(16))) unsigned char tile[32 * 528];
  int b = blockIdx.x;
  const int t = threadIdx.x;
  if (b < 3 * PREP_WQKV) {
    const int which = b % 3, r = b / 3, ks = r % 20, nc = r / 20;
    const u16* src = which == 0 ? a.wk : (which == 1 ? a.wv : a.wq);
    unsigned char* dst = a.ws + (which == 0 ? OFF_WKF : (which == 1 ? OFF_WVF : OFF_WQF));
    relayout_tile(src, NH * DH, ks, nc * 256, 256, dst, (size_t)nc * 16 * KS_D + ks, KS_D, false, tile);
    return;
  }
  b -= 3 * PREP_WQKV;
  if (b < PREP_WO) {
    const int kk = b / 3, nc = b % 3;
    relayout_tile(a.wo, DM, kk, nc * 256, nc == 2 ? 128 : 256, a.ws + OFF_WOF, ((size_t)(kk / 6) * 40 + nc * 16) * 6 + kk % 6, 6, true, tile);
    return;
  }
  b -= PREP_WO;
  if (b < PREP_LN) {
    const int lane = t & 63, w = t >> 6, r = lane >> 4, c = lane & 15;
    const int row = b * 16 + w * 4 + r;
    v4u v[5];
    float s = 0.f;
#pragma unroll
    for (int i = 0; i < 5; ++i) {
      v[i] = *(const v4u*)(a.x + (size_t)row * DM + (c + 16 * i) * 8);
      s += bflo(v[i].x) + bfhi(v[i].x) + bflo(v[i].y) + bfhi(v[i].y) + bflo(v[i].z) + bfhi(v[i].z) + bflo(v[i].w) + bfhi(v[i].w);
    }
#pragma unroll
    for (int o = 1; o < 16; o <<= 1) s += __shfl_xor(s, o);
    const float mean = s * (1.f / DM);
    float q = 0.f;
#pragma unroll
    for (int i = 0; i < 5; ++i) {
      const unsigned u[4] = {v[i].x, v[i].y, v[i].z, v[i].w};
#pragma unroll
      for (int j = 0; j < 4; ++j) { float d0 = bflo(u[j]) - mean, d1 = bfhi(u[j]) - mean; q += d0 * d0 + d1 * d1; }
    }
#pragma unroll
    for (int o = 1; o < 16; o <<= 1) q += __shfl_xor(q, o);
    const float rs = rsqrtf(q * (1.f / DM) + 1e-5f);
#pragma unroll
    for (int i = 0; i < 5; ++i) {
      const unsigned u[4] = {v[i].x, v[i].y, v[i].z, v[i].w};
      v4u o;
      o.x = cvtpk((bflo(u[0]) - mean) * rs, (bfhi(u[0]) - mean) * rs);
      o.y = cvtpk((bflo(u[1]) - mean) * rs, (bfhi(u[1]) - mean) * rs);
      o.z = cvtpk((bflo(u[2]) - mean) * rs, (bfhi(u[2]) - mean) * rs);
      o.w = cvtpk((bflo(u[3]) - mean) * rs, (bfhi(u[3]) - mean) * rs);
      const int ch = c + 16 * i;
      *(v4u*)(a.ws + OFF_XNF + ((size_t)(row >> 4) * KS_D + (ch >> 2)) * 1024 + ((ch & 3) * 16 + (row & 15)) * 16) = o;
    }
    return;
  }
  b -= PREP_LN;
  if (b < 2 * PREP_WGU) {
    const int which = b / PREP_WGU, r = b % PREP_WGU, ks = r % 20, nc = r / 20;
    relayout_tile(which ? a.wu : a.wg, DF, ks, nc * 256, 256, a.ws + (which ? OFF_WUF : OFF_WGF), (size_t)nc * 16 * KS_D + ks, KS_D, true, tile);
    return;
  }
  b -= 2 * PREP_WGU;
  if (b < PREP_WD) {
    const int ks = b / 3, nc = b % 3;
    relayout_tile(a.wd, DM, ks, nc * 256, nc == 2 ? 128 : 256, a.ws + OFF_WDF, (size_t)nc * 16 * KS_F + ks, KS_F, true, tile);
    return;
  }
  b -= PREP_WD;
  {
    constexpr int NSENT = (64 * 10240 + 64 * 20480) / 16;
    const v4u ones = {SENT, SENT, SENT, SENT}, zero = {0u, 0u, 0u, 0u};
    for (int i = b * 256 + t; i < NSENT; i += PREP_FILL * 256) *(v4u*)(a.ws + OFF_MEM + 10240 + (size_t)i * 16) = ones;
    for (int i = b * 256 + t; i < (int)(CTRL_BYTES / 16); i += PREP_FILL * 256) *(v4u*)(a.ws + OFF_CNT + (size_t)i * 16) = zero;
    if (b == 0) {
      for (int id = t; id < 640; id += 256) {
        const int nb = id >> 3, tok = id & 7;
        v4u v = *(const v4u*)(a.mem0 + tok * DM + nb * 8);
        v.x = desent(v.x); v.y = desent(v.y); v.z = desent(v.z); v.w = desent(v.w);
        *(v4u*)(a.ws + OFF_MEM + id * 16) = v;
      }
    }
  }
}

#define MFMA2_A(c0, c1, a, b0, b1) asm volatile("v_mfma_f32_16x16x32_bf16 %0, %2, %3, %0\n\tv_mfma_f32_16x16x32_bf16 %1, %2, %4, %1" \
      : "+a"(c0), "+a"(c1) : "v"(a), "v"(b0), "v"(b1))
#define MFMA2_V(c0, c1, a, b0, b1) asm volatile("v_mfma_f32_16x16x32_bf16 %0, %2, %3, %0\n\tv_mfma_f32_16x16x32_bf16 %1, %2, %4, %1" \
      : "+v"(c0), "+v"(c1) : "v"(a), "v"(b0), "v"(b1))
#define MFMA2B_V(c0, c1, a0, a1, b) asm volatile("v_mfma_f32_16x16x32_bf16 %0, %2, %4, %0\n\tv_mfma_f32_16x16x32_bf16 %1, %3, %4, %1" \
      : "+v"(c0), "+v"(c1) : "v"(a0), "v"(a1), "v"(b))
template <int N> __device__ __forceinline__ void wait_v() { asm volatile("s_waitcnt vmcnt(%0)" ::"n"(N) : "memory"); }

template <int NA>
__device__ __forceinline__ void proj_gemm(unsigned char* ws, unsigned char* smem, int w, int lane, int m, int h, unsigned wsel, f32x4 (&acc)[NA][2]) {
  constexpr int FPK = NA + 8, LPW = FPK / 4, BUFB = FPK * 1024, GI = NA / LPW;
#pragma unroll
  for (int a = 0; a < NA; ++a) { acc[a][0] = f32x4{0, 0, 0, 0}; acc[a][1] = f32x4{0, 0, 0, 0}; }
  unsigned soff[LPW];
#pragma unroll
  for (int i = 0; i < LPW; ++i) {
    const int ff = w * LPW + i;
    const unsigned base = NA == 12 ? wsel : (ff < 12 ? (unsigned)OFF_WQF : (ff < 24 ? (unsigned)OFF_WKF : (unsigned)OFF_WVF));
    const int et = ff % 12;
    const unsigned o = ff < NA ? base + (unsigned)(h * 12 + et) * (KS_D * 1024u) : (unsigned)OFF_XNF + (unsigned)(m * 8 + ff - NA) * (KS_D * 1024u);
    soff[i] = __builtin_amdgcn_readfirstlane(o);
  }
  const unsigned char* gl = ws + lane * 16;
#pragma unroll
  for (int st = 0; st < 3; ++st)
#pragma unroll
    for (int i = 0; i < LPW; ++i) glds16(gl + (soff[i] + (unsigned)st * 1024u), smem + st * BUFB + (w * LPW + i) * 1024);
  wait_v<2 * LPW>();
  RAW_BARRIER();
  v4u af[NA], b0, b1;
  {
    const unsigned char* buf = smem + lane * 16;
    b0 = *(const v4u*)(buf + (NA + 2 * w) * 1024); b1 = *(const v4u*)(buf + (NA + 2 * w + 1) * 1024);
#pragma unroll
    for (int a = 0; a < NA; ++a) af[a] = *(const v4u*)(buf + a * 1024);
  }
  int bn = 1;
#pragma unroll 1
  for (int i = 0; i < KS_D - 1; ++i) {
    wait_v<LPW>();
    RAW_BARRIER();
    const int bf = bn == 0 ? 2 : bn - 1;
    const unsigned nst = (unsigned)(i + 3 < KS_D ? i + 3 : KS_D - 1) * 1024u;
    const unsigned char* buf = smem + bn * BUFB + lane * 16;
    const v4u nb0 = *(const v4u*)(buf + (NA + 2 * w) * 1024), nb1 = *(const v4u*)(buf + (NA + 2 * w + 1) * 1024);
    asm volatile("s_nop 1");
#pragma unroll
    for (int a = 0; a < NA; ++a) {
      if (a < 32) MFMA2_A(acc[a][0], acc[a][1], af[a], b0, b1); else MFMA2_V(acc[a][0], acc[a][1], af[a], b0, b1);
      af[a] = *(const v4u*)(buf + a * 1024);
      if (a % GI == 1 && a / GI < LPW) glds16(gl + (soff[a / GI] + nst), smem + bf * BUFB + (w * LPW + a / GI) * 1024);
    }
    b0 = nb0; b1 = nb1;
    bn = bn == 2 ? 0 : bn + 1;
  }
  asm volatile("s_nop 1");
#pragma unroll
  for (int a = 0; a < NA; ++a) {
    if (a < 32) MFMA2_A(acc[a][0], acc[a][1], af[a], b0, b1); else MFMA2_V(acc[a][0], acc[a][1], af[a], b0, b1);
  }
  wait_v<0>();
  asm volatile("s_nop 7\n\ts_nop 7" ::: "memory");
#pragma unroll
  for (int a = 0; a < NA; ++a) { if (a < 32) asm volatile("" : "+a"(acc[a][0]), "+a"(acc[a][1])); else asm volatile("" : "+v"(acc[a][0]), "+v"(acc[a][1])); }
}

__device__ __forceinline__ void u_issue(unsigned char* ws, unsigned char* smem, int w, int lane, int h, int st, int buf) {
  const unsigned char* wo = ws + OFF_WOF + (size_t)h * 40 * 6 * 1024 + lane * 16;
#pragma unroll
  for (int i = 0; i < 6; ++i) glds16(wo + (size_t)(st * 24 + w * 6 + i) * 1024, smem + buf * 24576 + (w * 6 + i) * 1024);
}
template <int EPI, int PRE>
__device__ __forceinline__ void u_stage(unsigned char* ws, rsrc_t rs, unsigned char* smem, int w, int lane, int h, const v4u (&vf)[2][6],
                                        int st0, int nst, size_t obase) {
  wait_v<12 + PRE>();
  RAW_BARRIER();
  v4u bf[24];
#pragma unroll
  for (int i = 0; i < 24; ++i) bf[i] = *(const v4u*)(smem + i * 1024 + lane * 16);
  int bn = 1;
#pragma unroll 1
  for (int i0 = 0; i0 < nst; ++i0) {
    if (i0 == 0) { wait_v<6 + PRE>(); } else if (i0 == 1) { wait_v<10 + PRE>(); } else { wait_v<14>(); }
    RAW_BARRIER();
    const int bfree = bn == 0 ? 2 : bn - 1;
    u_issue(ws, smem, w, lane, h, st0 + (i0 + 3 < nst ? i0 + 3 : nst - 1), bfree);
    const unsigned char* buf = smem + bn * 24576 + lane * 16;
    f32x4 d0[4], d1[4];
#pragma unroll
    for (int i = 0; i < 4; ++i) { d0[i] = f32x4{0, 0, 0, 0}; d1[i] = f32x4{0, 0, 0, 0}; }
    asm volatile("s_nop 1" : "+v"(d0[0]), "+v"(d1[0]), "+v"(d0[1]), "+v"(d1[1]), "+v"(d0[2]), "+v"(d1[2]), "+v"(d0[3]), "+v"(d1[3]));
#pragma unroll
    for (int p = 0; p < 6; ++p)
#pragma unroll
      for (int i = 0; i < 4; ++i) {
        MFMA2B_V(d0[i], d1[i], vf[0][p], vf[1][p], bf[i * 6 + p]);
        bf[i * 6 + p] = *(const v4u*)(buf + (i * 6 + p) * 1024);
      }
    asm volatile("s_nop 7\n\ts_nop 1" : "+v"(d0[0]), "+v"(d1[0]), "+v"(d0[1]), "+v"(d1[1]), "+v"(d0[2]), "+v"(d1[2]), "+v"(d0[3]), "+v"(d1[3]));
    const int nt0 = (st0 + i0) * 4;
#pragma unroll
    for (int i = 0; i < 4; ++i) {
      if (EPI == 0) {
        st16(rs, obase + (size_t)(nt0 + i) * 2 * 16 * 1024, pack8(d0[i], d1[i]));
      } else {
        const v2u a = pack4(d0[i]), b = pack4(d1[i]);
        unsigned ax = a.x, ay = a.y, bx = b.x, by = b.y;
        swapu16(ax, bx); swapu16(ay, by);
        st16(rs, obase + (size_t)(nt0 + i) * 2 * 16 * 20 * 1024, v4u{ax, ay, bx, by});
      }
    }
    bn = bn == 2 ? 0 : bn + 1;
  }
}

template <int NB4>
__device__ __forceinline__ void store_qk(rsrc_t rs, size_t base, int lane, const f32x4 (&acc)[NB4][2], int a0) {
#pragma unroll
  for (int T = 0; T < 2; ++T)
#pragma unroll
    for (int gr = 0; gr < 4; ++gr) {
      const size_t p = base + (T * 4 + gr) * 1536;
      st16(rs, p + lane * 16, pack8(acc[a0 + 3 * gr][T], acc[a0 + 3 * gr + 1][T]));
      st8(rs, p + 1024 + lane * 8, pack4(acc[a0 + 3 * gr + 2][T]));
    }
}
__device__ __forceinline__ size_t ux_base(int seg, int h, int lane) {
  const int g = lane >> 4, l16 = lane & 15;
  return OFF_UX + ((size_t)(seg * 80 + (l16 >> 3)) * 16 + (h >> 1)) * 1024 + (g * 16 + (h & 1) * 8 + (l16 & 7)) * 16;
}

__device__ __forceinline__ void item_qkvu(unsigned char* ws, rsrc_t rs, unsigned char* smem, int w, int m, int h) {
  const int lane = lane_id();
  f32x4 acc[36][2];
  proj_gemm<36>(ws, smem, w, lane, m, h, 0u, acc);
  RAW_BARRIER();
  u_issue(ws, smem, w, lane, h, 0, 0); u_issue(ws, smem, w, lane, h, 1, 1); u_issue(ws, smem, w, lane, h, 2, 2);
  const int seg = m * 4 + w;
  v4u vf[2][6];
#pragma unroll
  for (int T = 0; T < 2; ++T)
#pragma unroll
    for (int p = 0; p < 6; ++p) vf[T][p] = pack8(acc[24 + 2 * p][T], acc[24 + 2 * p + 1][T]);
  store_qk<36>(rs, OFF_QX + (size_t)(seg * 32 + h) * 12288, lane, acc, 0);
  store_qk<36>(rs, OFF_KX + (size_t)(seg * 32 + h) * 12288, lane, acc, 12);
  u_stage<0, 32>(ws, rs, smem, w, lane, h, vf, 0, 10, ux_base(seg, h, lane));
}
__device__ __forceinline__ void item_piece(unsigned char* ws, rsrc_t rs, unsigned char* smem, int w, int m, int h, int kind) {
  const int lane = lane_id();
  f32x4 acc[12][2];
  proj_gemm<12>(ws, smem, w, lane, m, h, kind == 0 ? (unsigned)OFF_WQF : (kind == 1 ? (unsigned)OFF_WKF : (unsigned)OFF_WVF), acc);
  const int seg = m * 4 + w;
  if (kind < 2) {
    store_qk<12>(rs, (kind ? OFF_KX : OFF_QX) + (size_t)(seg * 32 + h) * 12288, lane, acc, 0);
  } else {
    const int st0 = (kind - 2) * 5;
    RAW_BARRIER();
    u_issue(ws, smem, w, lane, h, st0, 0); u_issue(ws, smem, w, lane, h, st0 + 1, 1); u_issue(ws, smem, w, lane, h, st0 + 2, 2);
    v4u vf[2][6];
#pragma unroll
    for (int T = 0; T < 2; ++T)
#pragma unroll
      for (int p = 0; p < 6; ++p) vf[T][p] = pack8(acc[2 * p][T], acc[2 * p + 1][T]);
    u_stage<0, 0>(ws, rs, smem, w, lane, h, vf, st0, 5, ux_base(seg, h, lane));
  }
}
__device__ __forceinline__ void item_wvo(const u16* wv, unsigned char* ws, rsrc_t rs, unsigned char* smem, int w, int h, int part) {
  const int lane = lane_id(), g = lane >> 4, l16 = lane & 15;
  const int ks = part * 4 + w;
  v4u vf[2][6];
#pragma unroll
  for (int T = 0; T < 2; ++T)
#pragma unroll
    for (int p = 0; p < 6; ++p) {
      const u16* src = wv + (size_t)(ks * 32 + T * 16 + l16) * (NH * DH) + h * DH + 32 * p + 4 * g;
      const v2u lo = *(const v2u*)src, hi = *(const v2u*)(src + 16);
      vf[T][p] = v4u{lo.x, lo.y, hi.x, hi.y};
    }
  wait_v<0>();
  u_issue(ws, smem, w, lane, h, 0, 0); u_issue(ws, smem, w, lane, h, 1, 1); u_issue(ws, smem, w, lane, h, 2, 2);
  const int gp = ((g & 1) << 1) | (g >> 1);
  const size_t ob = OFF_WVOF + ((size_t)((l16 >> 3) * 16 + (h >> 1)) * 20 + ks) * 1024 + (gp * 16 + (h & 1) * 8 + (l16 & 7)) * 16;
  u_stage<1, 0>(ws, rs, smem, w, lane, h, vf, 0, 10, ob);
}

__device__ __forceinline__ unsigned long long now() { return __builtin_amdgcn_s_memtime(); }
__device__ __forceinline__ void wait_until(unsigned long long t) { while ((long long)(now() - t) < 0) __builtin_amdgcn_s_sleep(1); }
#define DQ0 4250
#define DCM0 2250
#define DCP0 4050
#define T_DN 28
#define T_UP 72

__device__ __forceinline__ unsigned fetch_mem(__amdgpu_buffer_rsrc_t rs, int s, unsigned char* lmem, int tid) {
  const int base = (int)OFF_MEM + s * 10240;
  const int o0 = base + tid * 16, o1 = base + (tid + 256) * 16, o2 = base + ((tid & 127) + 512) * 16;
  v4u m0, m1, m2;
  unsigned spins = 0;
  for (; spins < SPIN_CAP; ++spins) {
    asm volatile("" ::: "memory");
    m0 = ld_sc1(rs, o0); m1 = ld_sc1(rs, o1); m2 = ld_sc1(rs, o2);
    const bool ok = !(isbad(m0) | isbad(m1) | isbad(m2));
    if (__all(ok)) break;
  }
  *(v4u*)(lmem + tid * 16) = m0;
  *(v4u*)(lmem + (tid + 256) * 16) = m1;
  *(v4u*)(lmem + ((tid & 127) + 512) * 16) = m2;
  return spins;
}

struct ChainT { int dq0, dcm0, dcp0, dn, up, pad; };
__device__ __forceinline__ void chain_role(const u16* wvin, unsigned char* ws, rsrc_t rs, unsigned char* smem, ChainT ct, int w) {
  unsigned char* lmem = smem;
  float* red = (float*)(smem + 10240);
  unsigned char* lw = smem + 10240 + 12288;
  if (blockIdx.x < NH) {
    const int h = blockIdx.x;
    item_piece(ws, rs, smem, w, 0, h, 1);
    signal_done(ws, w, CNT_M);
    const int lane = lane_id(), tid = w * 64 + lane, g = lane >> 4, l16 = lane & 15;
    const int bfo = g * 128 + (l16 & 7) * 16;
    v4u wa[60], wv[40];
    auto wsrc = [&](int ks, int tl) -> const unsigned char* {
      return ws + (tl < 3 ? OFF_WQF : OFF_WKF) + ((size_t)(h * 12 + 3 * w + (tl % 3)) * KS_D + ks) * 1024 + lane * 16;
    };
#pragma unroll
    for (int ks = 0; ks < KS_D; ++ks) {
#pragma unroll
      for (int t = 0; t < 3; ++t) wa[ks * 3 + t] = *(const v4u*)wsrc(ks, t);
#pragma unroll
      for (int t = 0; t < 2; ++t) wv[ks * 2 + t] = *(const v4u*)wsrc(ks, 3 + t);
      *(v4u*)(lw + (w * KS_D + ks) * 1024 + lane * 16) = *(const v4u*)wsrc(ks, 5);
    }
    const unsigned char* lwl = lw + w * KS_D * 1024 + lane * 16;
    const float scale = 0.07216878364870322f * 1.4426950408889634f;
    unsigned long long tref = 0; int dq = ct.dq0;
    for (int s = 0; s < NSEG; ++s) {
      if ((s & 3) == 0) wave_wait_count(ws, CNT_M + (s >> 2), m_target(s >> 2), true);
      const int kx = (int)OFF_KX + (s * 32 + h) * 12288 + w * 1536;
      const v4u kp0 = ld_sc1(rs, kx + lane * 16), kp1 = ld_sc1(rs, kx + 4 * 1536 + lane * 16);
      const v2u ks0 = ld8_sc1(rs, kx + 1024 + lane * 8), ks1 = ld8_sc1(rs, kx + 4 * 1536 + 1024 + lane * 8);
      const int qx = (int)OFF_QX + (s * 32 + h) * 12288 + w * 1536;
      v4u wl[3];
      wl[0] = *(const v4u*)(lwl); wl[1] = *(const v4u*)(lwl + 1024);
      if (s > 0) wait_until(tref + dq);
      { const unsigned sp = fetch_mem(rs, s, lmem, tid); if (s > 0) dq += sp ? ct.up : -ct.dn;
      }
      __syncthreads();
      if (s > 0 && tid == 64) set_flag(ws, s - 1, h);
      f32x4 acc[6];
#pragma unroll
      for (int t = 0; t < 6; ++t) acc[t] = f32x4{0, 0, 0, 0};
      v4u bq[3];
      bq[0] = *(const v4u*)(lmem + bfo); bq[1] = *(const v4u*)(lmem + 512 + bfo);
#pragma unroll
      for (int ks = 0; ks < KS_D; ++ks) {
        if (ks + 2 < KS_D) {
          bq[(ks + 2) % 3] = *(const v4u*)(lmem + (ks + 2) * 512 + bfo);
          wl[(ks + 2) % 3] = *(const v4u*)(lwl + (ks + 2) * 1024);
        }
        if (ks == 0) asm volatile("s_nop 1" : "+v"(acc[0]), "+v"(acc[1]), "+v"(acc[2]), "+v"(acc[3]), "+v"(acc[4]), "+v"(acc[5]));
        asm volatile(
            "v_mfma_f32_16x16x32_bf16 %0, %6, %12, %0\n\t"
            "v_mfma_f32_16x16x32_bf16 %1, %7, %12, %1\n\t"
            "v_mfma_f32_16x16x32_bf16 %2, %8, %12, %2\n\t"
            "v_mfma_f32_16x16x32_bf16 %3, %9, %12, %3\n\t"
            "v_mfma_f32_16x16x32_bf16 %4, %10, %12, %4\n\t"
            "v_mfma_f32_16x16x32_bf16 %5, %11, %12, %5"
            : "+v"(acc[0]), "+v"(acc[1]), "+v"(acc[2]), "+v"(acc[3]), "+v"(acc[4]), "+v"(acc[5])
            : "a"(wa[ks * 3]), "a"(wa[ks * 3 + 1]), "a"(wa[ks * 3 + 2]), "v"(wv[ks * 2]), "v"(wv[ks * 2 + 1]), "v"(wl[ks % 3]), "v"(bq[ks % 3]));
      }
      asm volatile("s_nop 7\n\ts_nop 1" : "+v"(acc[0]), "+v"(acc[1]), "+v"(acc[2]), "+v"(acc[3]), "+v"(acc[4]), "+v"(acc[5]));
      const v4u qp = pack8(acc[0], acc[1]);
      const v2u qs2 = pack4(acc[2]);
      const v4u qs = {qs2.x, qs2.y, 0u, 0u};
      const v4u kmp = pack8(acc[3], acc[4]);
      const v2u kms2 = pack4(acc[5]);
      const v4u kms = {kms2.x, kms2.y, 0u, 0u};
      f32x4 z = {0, 0, 0, 0};
      f32x4 s0 = mfma(kmp, qp, z); s0 = mfma(kms, qs, s0);
      f32x4 s1 = mfma(kp0, qp, z); s1 = mfma(v4u{ks0.x, ks0.y, 0u, 0u}, qs, s1);
      f32x4 s2 = mfma(kp1, qp, z); s2 = mfma(v4u{ks1.x, ks1.y, 0u, 0u}, qs, s2);
      if (w != 0) {
        *(f32x4*)(red + ((w * 3 + 0) * 64 + lane) * 4) = s0;
        *(f32x4*)(red + ((w * 3 + 1) * 64 + lane) * 4) = s1;
        *(f32x4*)(red + ((w * 3 + 2) * 64 + lane) * 4) = s2;
      }
      __syncthreads();
      tref = now();
      if (w == 0) {
        f32x4 r_[9];
#pragma unroll
        for (int i = 0; i < 9; ++i) r_[i] = *(const f32x4*)(red + ((3 + i) * 64 + lane) * 4);
        __builtin_amdgcn_sched_barrier(0);
        f32x4 t0 = (s0 + r_[0]) + (r_[3] + r_[6]), t1 = (s1 + r_[1]) + (r_[4] + r_[7]), t2 = (s2 + r_[2]) + (r_[5] + r_[8]);
        const bool mv = g < 2;
        if (!mv) t0 = t1;
        float mx = fmaxf(fmaxf(fmaxf(t1.x, t1.y), fmaxf(t1.z, t1.w)), fmaxf(fmaxf(t2.x, t2.y), fmaxf(t2.z, t2.w)));
        mx = fmaxf(mx, fmaxf(fmaxf(t0.x, t0.y), fmaxf(t0.z, t0.w)));
        mx = xmax32(xmax16(mx));
        const float mxs = mx * scale;
        f32x4 e0, e1, e2;
        e0.x = __builtin_amdgcn_exp2f(t0.x * scale - mxs); e0.y = __builtin_amdgcn_exp2f(t0.y * scale - mxs);
        e0.z = __builtin_amdgcn_exp2f(t0.z * scale - mxs); e0.w = __builtin_amdgcn_exp2f(t0.w * scale - mxs);
        e1.x = __builtin_amdgcn_exp2f(t1.x * scale - mxs); e1.y = __builtin_amdgcn_exp2f(t1.y * scale - mxs);
        e1.z = __builtin_amdgcn_exp2f(t1.z * scale - mxs); e1.w = __builtin_amdgcn_exp2f(t1.w * scale - mxs);
        e2.x = __builtin_amdgcn_exp2f(t2.x * scale - mxs); e2.y = __builtin_amdgcn_exp2f(t2.y * scale - mxs);
        e2.z = __builtin_amdgcn_exp2f(t2.z * scale - mxs); e2.w = __builtin_amdgcn_exp2f(t2.w * scale - mxs);
        if (!mv) e0 = z;
        float sum = ((e0.x + e0.y) + (e0.z + e0.w)) + ((e1.x + e1.y) + (e1.z + e1.w)) + ((e2.x + e2.y) + (e2.z + e2.w));
        sum = xadd32(xadd16(sum));
        const float inv = __builtin_amdgcn_rcpf(sum);
        e0 *= inv; e1 *= inv; e2 *= inv;
        v4u px = pack8(e1, e2);
        v2u pm = pack4(e0);
        px.x &= 0x7FFF7FFFu; px.y &= 0x7FFF7FFFu; px.z &= 0x7FFF7FFFu; px.w &= 0x7FFF7FFFu;
        pm.x &= 0x7FFF7FFFu; pm.y &= 0x7FFF7FFFu;
        const int pb = (int)OFF_P + s * 20480 + (h >> 3) * 5120;
        if (l16 < 8) {
          __builtin_amdgcn_raw_buffer_store_b128(px, rs, pb + ((h >> 1) & 3) * 1024 + (g * 16 + (h & 1) * 8 + l16) * 16, 0, 16);
          if (mv) __builtin_amdgcn_raw_buffer_store_b64(pm, rs, pb + 4096 + (((h >> 1) & 3) * 16 + (h & 1) * 8 + l16) * 16 + g * 8, 0, 16);
        }
      }
      __builtin_amdgcn_s_barrier();
      {
        if (s == 0 || s == 4) wave_wait_count(ws, CNT_Q0 + (s >> 2), 32, true);
        const v4u xp0 = ld_sc1(rs, qx + lane * 16), xp1 = ld_sc1(rs, qx + 4 * 1536 + lane * 16);
        const v2u xs0 = ld8_sc1(rs, qx + 1024 + lane * 8), xs1 = ld8_sc1(rs, qx + 4 * 1536 + 1024 + lane * 8);
        const v4u xq0 = {xs0.x, xs0.y, 0u, 0u}, xq1 = {xs1.x, xs1.y, 0u, 0u};
        const v4u k0s = {ks0.x, ks0.y, 0u, 0u}, k1s = {ks1.x, ks1.y, 0u, 0u};
        f32x4* red2 = (f32x4*)(smem + 10240 + 12288 + 81920);
        red2[((w * 2 + 0) * 3 + 0) * 64 + lane] = mfma(kms, xq0, mfma(kmp, xp0, z));
        red2[((w * 2 + 0) * 3 + 1) * 64 + lane] = mfma(k0s, xq0, mfma(kp0, xp0, z));
        red2[((w * 2 + 0) * 3 + 2) * 64 + lane] = mfma(k1s, xq0, mfma(kp1, xp0, z));
        red2[((w * 2 + 1) * 3 + 0) * 64 + lane] = mfma(kms, xq1, mfma(kmp, xp1, z));
        red2[((w * 2 + 1) * 3 + 1) * 64 + lane] = mfma(k0s, xq1, mfma(kp0, xp1, z));
        red2[((w * 2 + 1) * 3 + 2) * 64 + lane] = mfma(k1s, xq1, mfma(kp1, xp1, z));
        __syncthreads();
        if (w >= 2) {
          const int T = w & 1;
          f32x4 t[3];
#pragma unroll
          for (int kt = 0; kt < 3; ++kt)
            t[kt] = (red2[((0 * 2 + T) * 3 + kt) * 64 + lane] + red2[((1 * 2 + T) * 3 + kt) * 64 + lane]) +
                    (red2[((2 * 2 + T) * 3 + kt) * 64 + lane] + red2[((3 * 2 + T) * 3 + kt) * 64 + lane]);
          const int tq = 16 * T + l16;
          const float NEG = -1e30f;
          float v[12];
#pragma unroll
          for (int r = 0; r < 4; ++r) {
            v[r] = g < 2 ? t[0][r] * scale : NEG;
            v[4 + r] = (4 * g + r <= tq) ? t[1][r] * scale : NEG;
            v[8 + r] = (16 + 4 * g + r <= tq) ? t[2][r] * scale : NEG;
          }
          float mx = v[0];
#pragma unroll
          for (int i = 1; i < 12; ++i) mx = fmaxf(mx, v[i]);
          mx = xmax32(xmax16(mx));
          float sum = 0.f;
#pragma unroll
          for (int i = 0; i < 12; ++i) { v[i] = __builtin_amdgcn_exp2f(v[i] - mx); sum += v[i]; }
          sum = xadd32(xadd16(sum));
          const float inv = __builtin_amdgcn_rcpf(sum);
          const size_t pxb = OFF_PX + (size_t)s * 81920;
          st16(rs, pxb + (h * 2 + T) * 1024 + lane * 16,
               v4u{cvtpk(v[4] * inv, v[5] * inv), cvtpk(v[6] * inv, v[7] * inv), cvtpk(v[8] * inv, v[9] * inv), cvtpk(v[10] * inv, v[11] * inv)});
          if (g < 2) st8(rs, pxb + 65536 + ((h >> 2) * 2 + T) * 1024 + ((h & 3) * 16 + l16) * 16 + g * 8,
                         v2u{cvtpk(v[0] * inv, v[1] * inv), cvtpk(v[2] * inv, v[3] * inv)});
        }
      }
    }
    asm volatile("s_waitcnt vmcnt(0)" ::: "memory");
    __syncthreads();
    if (tid == 0) set_flag(ws, NSEG - 1, h);
  } else if (blockIdx.x < NH + NBK) {
    const int nb = blockIdx.x - NH;
    item_wvo(wvin, ws, rs, smem, w, nb / 5, nb % 5);
    signal_done(ws, w, CNT_WVO);
    wait_count(ws, w, CNT_WVO, 160);
    const int lane = lane_id(), tid = w * 64 + lane, g = lane >> 4, l16 = lane & 15;
    const int bfo = g * 128 + (l16 & 7) * 16;
    v4u wa[60], wv[20];
#pragma unroll
    for (int ks = 0; ks < KS_D; ++ks) {
#pragma unroll
      for (int t = 0; t < 3; ++t)
        wa[ks * 3 + t] = *(const v4u*)(ws + OFF_WVOF + ((size_t)(nb * 16 + 4 * w + t) * 20 + ks) * 1024 + lane * 16);
      wv[ks] = *(const v4u*)(ws + OFF_WVOF + ((size_t)(nb * 16 + 4 * w + 3) * 20 + ks) * 1024 + lane * 16);
    }
    asm volatile("s_waitcnt vmcnt(0)" ::: "memory");
    unsigned char* lu = lw + w * 1024;
    unsigned long long tref = 0, tmem = 0; int dcm = ct.dcm0, dcp = ct.dcp0;
    for (int s = 0; s < NSEG; ++s) {
      if ((s & 3) == 0) wave_wait_count(ws, CNT_M + (s >> 2), m_target(s >> 2), true);
      v4u ux[4];
#pragma unroll
      for (int t = 0; t < 4; ++t) ux[t] = ld_sc1(rs, (int)OFF_UX + ((s * 80 + nb) * 16 + 4 * w + t) * 1024 + lane * 16);
      if (s > 0) wait_until(tref + dcm);
      { const unsigned sp = fetch_mem(rs, s, lmem, tid); if (s > 0) dcm += sp ? ct.up : -ct.dn; }
      __syncthreads();
      tmem = now();
      f32x4 acc[4];
#pragma unroll
      for (int t = 0; t < 4; ++t) acc[t] = f32x4{0, 0, 0, 0};
      v4u aq[3];
      aq[0] = *(const v4u*)(lmem + bfo); aq[1] = *(const v4u*)(lmem + 512 + bfo);
#pragma unroll
      for (int ks = 0; ks < KS_D; ++ks) {
        if (ks + 2 < KS_D) aq[(ks + 2) % 3] = *(const v4u*)(lmem + (ks + 2) * 512 + bfo);
        if (ks == 0) asm volatile("s_nop 1" : "+v"(acc[0]), "+v"(acc[1]), "+v"(acc[2]), "+v"(acc[3]));
        asm volatile(
            "v_mfma_f32_16x16x32_bf16 %0, %8, %4, %0\n\t"
            "v_mfma_f32_16x16x32_bf16 %1, %8, %5, %1\n\t"
            "v_mfma_f32_16x16x32_bf16 %2, %8, %6, %2\n\t"
            "v_mfma_f32_16x16x32_bf16 %3, %8, %7, %3"
            : "+v"(acc[0]), "+v"(acc[1]), "+v"(acc[2]), "+v"(acc[3])
            : "a"(wa[ks * 3]), "a"(wa[ks * 3 + 1]), "a"(wa[ks * 3 + 2]), "v"(wv[ks]), "v"(aq[ks % 3]));
      }
      asm volatile("s_nop 7\n\ts_nop 1" : "+v"(acc[0]), "+v"(acc[1]), "+v"(acc[2]), "+v"(acc[3]));
      if (g < 2) {
#pragma unroll
        for (int t = 0; t < 4; ++t) *(v2u*)(lu + (t * 16 + l16) * 16 + g * 8) = pack4(acc[t]);
      }
      asm volatile("s_waitcnt lgkmcnt(0)" ::: "memory");
      const v4u um = *(const v4u*)(lu + (g * 16 + l16) * 16);
      st16(rs, OFF_UM + ((size_t)(s * 32 + 8 * w + 2 * g + (l16 >> 3)) * 640 + 8 * nb + (l16 & 7)) * 16, um);
      const int pb = (int)OFF_P + s * 20480 + w * 5120 + lane * 16;
      v4u p0, p1, p2, p3, p4;
      wait_until(tmem + dcp);
      unsigned spins = 0;
      for (; spins < SPIN_CAP; ++spins) {
        asm volatile("" ::: "memory");
        p0 = ld_sc1(rs, pb); p1 = ld_sc1(rs, pb + 1024); p2 = ld_sc1(rs, pb + 2048); p3 = ld_sc1(rs, pb + 3072); p4 = ld_sc1(rs, pb + 4096);
        const bool ok = !(isbad(p0) | isbad(p1) | isbad(p2) | isbad(p3) | isbad(p4));
        if (__all(ok)) break;
      }
      dcp += spins ? ct.up : -ct.dn;
      f32x4 d = {0, 0, 0, 0}, d2 = {0, 0, 0, 0};
      d = mfma(ux[0], p0, d); d2 = mfma(ux[1], p1, d2); d = mfma(ux[2], p2, d); d2 = mfma(ux[3], p3, d2); d = mfma(um, p4, d);
      d += d2;
      if ((l16 < 8) == (g < 2)) *(f32x4*)(red + (((w * 2 + (g >> 1)) * 8 + (l16 & 7)) * 8 + (g & 1) * 4)) = d;
      __syncthreads();
      tref = now();
      if (tid == 64) set_flag(ws, s, 32 + nb);
      if (w == 0) {
        float r_[8];
#pragma unroll
        for (int q = 0; q < 8; ++q) r_[q] = red[q * 64 + lane];
        __builtin_amdgcn_sched_barrier(0);
        const float v = ((r_[0] + r_[1]) + (r_[2] + r_[3])) + ((r_[4] + r_[5]) + (r_[6] + r_[7]));
        const float vn = __builtin_bit_cast(float, __builtin_amdgcn_mov_dpp(__builtin_bit_cast(int, v), 0xB1, 0xF, 0xF, true));
        const unsigned d0 = desent(cvtpk(v, vn));
        const unsigned d1 = (unsigned)__builtin_amdgcn_mov_dpp((int)d0, 0x4E, 0xF, 0xF, true);
        const unsigned e0 = (unsigned)__builtin_amdgcn_mov_dpp((int)d0, 0x104, 0xF, 0xF, true);
        const unsigned e1 = (unsigned)__builtin_amdgcn_mov_dpp((int)d1, 0x104, 0xF, 0xF, true);
        if ((lane & 7) == 0)
          __builtin_amdgcn_raw_buffer_store_b128(v4u{d0, d1, e0, e1}, rs, (int)OFF_MEM + (s + 1) * 10240 + nb * 128 + (lane >> 3) * 16, 0, 16);
      }
    }
  }
}

__device__ __forceinline__ void item_pb(const u16* x, unsigned char* ws, rsrc_t rs, unsigned char* smem, int w, int seg, int part) {
  const int lane = lane_id(), g = lane >> 4, l16 = lane & 15;
  const int pxb = (int)OFF_PX + seg * 81920 + lane * 16;
  v4u pf[10][2], uf[10][4];
  wave_wait_count(ws, CNT_M + (seg >> 2), m_target(seg >> 2));
#pragma unroll
  for (int i = 0; i < 8; ++i) {
    const int h = 8 * w + i;
#pragma unroll
    for (int c = 0; c < 4; ++c)
      uf[i][c] = ld_sc1(rs, (int)OFF_UX + ((seg * 80 + 2 * (part * 4 + c) + (l16 >> 3)) * 16 + (h >> 1)) * 1024 + (g * 16 + (h & 1) * 8 + (l16 & 7)) * 16);
  }
  const int pr = w >> 1, Tf = w & 1;
  v2u xv[2];
#pragma unroll
  for (int i = 0; i < 2; ++i) xv[i] = *(const v2u*)(x + (size_t)(seg * 32 + 16 * Tf + l16) * DM + 16 * (part * 4 + 2 * pr + i) + 4 * g);
  __builtin_amdgcn_sched_barrier(0);
  wave_wait_flags(ws, seg, 8 * w, 8, lane, seg >= FAST_SEG);
  wave_wait_flags(ws, seg, 32 + 8 * part, 8, lane, seg >= FAST_SEG);
#pragma unroll
  for (int i = 0; i < 8; ++i)
#pragma unroll
    for (int T = 0; T < 2; ++T) pf[i][T] = ld_sc1(rs, pxb + ((8 * w + i) * 2 + T) * 1024);
#pragma unroll
  for (int i = 0; i < 2; ++i) {
    const int hq = 2 * w + i;
#pragma unroll
    for (int T = 0; T < 2; ++T) pf[8 + i][T] = ld_sc1(rs, pxb + 65536 + (hq * 2 + T) * 1024);
#pragma unroll
    for (int c = 0; c < 4; ++c)
      uf[8 + i][c] = ld_sc1(rs, (int)OFF_UM + ((seg * 32 + 4 * hq + g) * 640 + 16 * (part * 4 + c) + l16) * 16);
  }
  __builtin_amdgcn_sched_barrier(0);
  f32x4 acc[4][2];
#pragma unroll
  for (int c = 0; c < 4; ++c) { acc[c][0] = f32x4{0, 0, 0, 0}; acc[c][1] = f32x4{0, 0, 0, 0}; }
#pragma unroll
  for (int i = 0; i < 10; ++i)
#pragma unroll
    for (int c = 0; c < 4; ++c) {
      acc[c][0] = mfma(uf[i][c], pf[i][0], acc[c][0]);
      acc[c][1] = mfma(uf[i][c], pf[i][1], acc[c][1]);
    }
  f32x4* red = (f32x4*)smem;
#pragma unroll
  for (int c = 0; c < 4; ++c)
#pragma unroll
    for (int T = 0; T < 2; ++T) red[((w * 4 + c) * 2 + T) * 64 + lane] = acc[c][T];
  __syncthreads();
  {
    f32x4 o[2];
#pragma unroll
    for (int i = 0; i < 2; ++i) {
      const int c = 2 * pr + i;
      o[i] = (red[((0 * 4 + c) * 2 + Tf) * 64 + lane] + red[((1 * 4 + c) * 2 + Tf) * 64 + lane]) +
             (red[((2 * 4 + c) * 2 + Tf) * 64 + lane] + red[((3 * 4 + c) * 2 + Tf) * 64 + lane]);
      o[i].x = bflo(xv[i].x) + rbf(o[i].x); o[i].y = bfhi(xv[i].x) + rbf(o[i].y);
      o[i].z = bflo(xv[i].y) + rbf(o[i].z); o[i].w = bfhi(xv[i].y) + rbf(o[i].w);
    }
    st16(rs, OFF_HF + ((size_t)(seg * 2 + Tf) * KS_D + part * 2 + pr) * 1024 + lane * 16, pack8(o[0], o[1]));
  }
}

__device__ __forceinline__ float gelu_tanh(float x) {
  const float u = 0.7978845608028654f * (x + 0.044715f * x * x * x);
  const float e = __expf(2.f * u);
  const float t = 1.f - 2.f / (1.f + e);
  return 0.5f * x * (1.f + t);
}

__device__ __forceinline__ void item_f1(unsigned char* ws, rsrc_t rs, unsigned char* smem, int w, int seg, int part) {
  const int lane = lane_id(), g = lane >> 4, l16 = lane & 15;
  const unsigned char* wg = ws + OFF_WGF + (size_t)(part * 8 + 2 * w) * KS_D * 1024 + lane * 16;
  const unsigned char* wu = ws + OFF_WUF + (size_t)(part * 8 + 2 * w) * KS_D * 1024 + lane * 16;
  v4u wf[KS_D][4];
#pragma unroll
  for (int ks = 0; ks < KS_D; ++ks) {
    wf[ks][0] = *(const v4u*)(wg + ks * 1024); wf[ks][1] = *(const v4u*)(wg + (KS_D + ks) * 1024);
    wf[ks][2] = *(const v4u*)(wu + ks * 1024); wf[ks][3] = *(const v4u*)(wu + (KS_D + ks) * 1024);
  }
  __builtin_amdgcn_sched_barrier(0);
  wave_wait_count(ws, CNT_PB + seg, 10, seg >= FAST_SEG);
  v4u hv[2][5];
#pragma unroll
  for (int T = 0; T < 2; ++T)
#pragma unroll
    for (int i = 0; i < 5; ++i) hv[T][i] = ld_sc1(rs, (int)OFF_HF + ((seg * 2 + T) * KS_D + 5 * w + i) * 1024 + lane * 16);
  __builtin_amdgcn_sched_barrier(0);
  float* lred = (float*)(smem + 40960);
  float mean[2], rstd[2];
#pragma unroll
  for (int T = 0; T < 2; ++T) {
    float sacc = 0.f;
#pragma unroll
    for (int i = 0; i < 5; ++i) {
      const v4u u = hv[T][i];
      sacc += (bflo(u.x) + bfhi(u.x)) + (bflo(u.y) + bfhi(u.y)) + (bflo(u.z) + bfhi(u.z)) + (bflo(u.w) + bfhi(u.w));
    }
    sacc = xadd32(xadd16(sacc));
    if (g == 0) lred[w * 32 + 16 * T + l16] = sacc;
  }
  __syncthreads();
#pragma unroll
  for (int T = 0; T < 2; ++T) {
    mean[T] = ((lred[16 * T + l16] + lred[32 + 16 * T + l16]) + (lred[64 + 16 * T + l16] + lred[96 + 16 * T + l16])) * (1.f / DM);
    float q = 0.f;
#pragma unroll
    for (int i = 0; i < 5; ++i) {
      const unsigned u[4] = {hv[T][i].x, hv[T][i].y, hv[T][i].z, hv[T][i].w};
#pragma unroll
      for (int j = 0; j < 4; ++j) { const float d0 = bflo(u[j]) - mean[T], d1 = bfhi(u[j]) - mean[T]; q += d0 * d0 + d1 * d1; }
    }
    q = xadd32(xadd16(q));
    if (g == 0) lred[128 + w * 32 + 16 * T + l16] = q;
  }
  __syncthreads();
#pragma unroll
  for (int T = 0; T < 2; ++T) {
    const float vv = ((lred[128 + 16 * T + l16] + lred[160 + 16 * T + l16]) + (lred[192 + 16 * T + l16] + lred[224 + 16 * T + l16])) * (1.f / DM);
    rstd[T] = rsqrtf(vv + 1e-5f);
#pragma unroll
    for (int i = 0; i < 5; ++i) {
      const unsigned u[4] = {hv[T][i].x, hv[T][i].y, hv[T][i].z, hv[T][i].w};
      v4u o;
      o.x = cvtpk((bflo(u[0]) - mean[T]) * rstd[T], (bfhi(u[0]) - mean[T]) * rstd[T]);
      o.y = cvtpk((bflo(u[1]) - mean[T]) * rstd[T], (bfhi(u[1]) - mean[T]) * rstd[T]);
      o.z = cvtpk((bflo(u[2]) - mean[T]) * rstd[T], (bfhi(u[2]) - mean[T]) * rstd[T]);
      o.w = cvtpk((bflo(u[3]) - mean[T]) * rstd[T], (bfhi(u[3]) - mean[T]) * rstd[T]);
      *(v4u*)(smem + (T * KS_D + 5 * w + i) * 1024 + lane * 16) = o;
    }
  }
  __syncthreads();
  f32x4 acc[4][2];
#pragma unroll
  for (int a = 0; a < 4; ++a) { acc[a][0] = f32x4{0, 0, 0, 0}; acc[a][1] = f32x4{0, 0, 0, 0}; }
#pragma unroll
  for (int ks = 0; ks < KS_D; ++ks) {
    const v4u b0 = *(const v4u*)(smem + ks * 1024 + lane * 16), b1 = *(const v4u*)(smem + (KS_D + ks) * 1024 + lane * 16);
#pragma unroll
    for (int a = 0; a < 4; ++a) { acc[a][0] = mfma(wf[ks][a], b0, acc[a][0]); acc[a][1] = mfma(wf[ks][a], b1, acc[a][1]); }
  }
#pragma unroll
  for (int T = 0; T < 2; ++T) {
    f32x4 o[2];
#pragma unroll
    for (int i = 0; i < 2; ++i)
#pragma unroll
      for (int r = 0; r < 4; ++r) {
        const float gg = rbf(acc[i][T][r]), uu = rbf(acc[2 + i][T][r]);
        o[i][r] = rbf(gelu_tanh(gg)) * uu;
      }
    st16(rs, OFF_ACTF + ((size_t)(seg * 2 + T) * KS_F + part * 4 + w) * 1024 + lane * 16, pack8(o[0], o[1]));
  }
}

__device__ __forceinline__ void item_f2(unsigned char* ws, rsrc_t rs, unsigned char* smem, u16* out, int w, int seg, int part) {
  const int lane = lane_id(), g = lane >> 4, l16 = lane & 15;
  v4u af[12][4], bf[12][2];
#pragma unroll
  for (int i = 0; i < 12; ++i)
#pragma unroll
    for (int c = 0; c < 4; ++c) af[i][c] = *(const v4u*)(ws + OFF_WDF + ((size_t)(part * 4 + c) * KS_F + 12 * w + i) * 1024 + lane * 16);
  __builtin_amdgcn_sched_barrier(0);
  wave_wait_count(ws, CNT_F1 + seg, 12, seg >= FAST_SEG);
#pragma unroll
  for (int i = 0; i < 12; ++i)
#pragma unroll
    for (int T = 0; T < 2; ++T) bf[i][T] = ld_sc1(rs, (int)OFF_ACTF + ((seg * 2 + T) * KS_F + 12 * w + i) * 1024 + lane * 16);
  const int pr = w >> 1, Tf = w & 1;
  const v4u hres = ld_sc1(rs, (int)OFF_HF + ((seg * 2 + Tf) * KS_D + part * 2 + pr) * 1024 + lane * 16);
  __builtin_amdgcn_sched_barrier(0);
  f32x4 acc[4][2];
#pragma unroll
  for (int c = 0; c < 4; ++c) { acc[c][0] = f32x4{0, 0, 0, 0}; acc[c][1] = f32x4{0, 0, 0, 0}; }
#pragma unroll
  for (int i = 0; i < 12; ++i)
#pragma unroll
    for (int c = 0; c < 4; ++c) { acc[c][0] = mfma(af[i][c], bf[i][0], acc[c][0]); acc[c][1] = mfma(af[i][c], bf[i][1], acc[c][1]); }
  f32x4* red = (f32x4*)smem;
#pragma unroll
  for (int c = 0; c < 4; ++c)
#pragma unroll
    for (int T = 0; T < 2; ++T) red[((w * 4 + c) * 2 + T) * 64 + lane] = acc[c][T];
  __syncthreads();
  {
    const unsigned hr[4] = {hres.x, hres.y, hres.z, hres.w};
    const int tok = seg * 32 + 16 * Tf + l16;
#pragma unroll
    for (int i = 0; i < 2; ++i) {
      const int c = 2 * pr + i;
      f32x4 o = (red[((0 * 4 + c) * 2 + Tf) * 64 + lane] + red[((1 * 4 + c) * 2 + Tf) * 64 + lane]) +
                (red[((2 * 4 + c) * 2 + Tf) * 64 + lane] + red[((3 * 4 + c) * 2 + Tf) * 64 + lane]);
      o.x = bflo(hr[2 * i]) + rbf(o.x); o.y = bfhi(hr[2 * i]) + rbf(o.y);
      o.z = bflo(hr[2 * i + 1]) + rbf(o.z); o.w = bfhi(hr[2 * i + 1]) + rbf(o.w);
      *(v2u*)(out + (size_t)tok * DM + 16 * (part * 4 + c) + 4 * g) = pack4(o);
    }
  }
  __syncthreads();
}

constexpr int NCHAIN = NH + NBK, NWORK = 256 - NCHAIN;
constexpr int IT_QKR = 448, IT_PRE = 160 + IT_QKR, IT_ROUND = 32, N_ROUNDS = NSEG + 2;
constexpr int IT_TOTAL = IT_PRE + N_ROUNDS * IT_ROUND;
struct MainArgs { const u16* x; const u16* wv; unsigned char* ws; u16* out; ChainT ct; };

__global__ void __launch_bounds__(256, 1) k_main(MainArgs a) {
  extern __shared__ __attribute__((aligned(16))) unsigned char smem[];
  unsigned char* ws = a.ws;
  const rsrc_t rs = __builtin_amdgcn_make_buffer_rsrc((void*)ws, 0, (int)WS_TOTAL, 0x00020000);
  const int w = __builtin_amdgcn_readfirstlane(threadIdx.x >> 6);
  if (blockIdx.x < NCHAIN) { chain_role(a.wv, ws, rs, smem, a.ct, w); return; }
  const int wk = blockIdx.x - NCHAIN;
  if (wk < 80) {
    item_wvo(a.wv, ws, rs, smem, w, (80 + wk) / 5, (80 + wk) % 5);
    signal_done(ws, w, CNT_WVO);
  } else {
    item_piece(ws, rs, smem, w, 0, (wk - 80) & 31, 2 + ((wk - 80) >> 5));
    signal_done(ws, w, CNT_M);
  }
  int it = wk;
  for (; it < IT_PRE; it += NWORK) {
    if (it < 32) {
      item_piece(ws, rs, smem, w, 0, it, 0);
      signal_done(ws, w, CNT_Q0);
    } else if (it < 160) {
      const int j = it - 32;
      item_piece(ws, rs, smem, w, 1, j & 31, j < 64 ? 2 + (j >> 5) : (j < 96 ? 1 : 0));
      signal_done(ws, w, j < 96 ? CNT_M + 1 : CNT_Q0 + 1);
    } else {
      const int j = it - 160 + 64;
      item_qkvu(ws, rs, smem, w, j >> 5, j & 31);
      signal_done(ws, w, CNT_M + (j >> 5));
    }
  }
  for (; it < IT_TOTAL; it += NWORK) {
    const int j = it - IT_PRE, r = j / IT_ROUND, q = j % IT_ROUND;
    if (q < 10) {
      const int seg = r;
      if (seg >= NSEG) continue;
      item_pb(a.x, ws, rs, smem, w, seg, q);
      signal_done(ws, w, CNT_PB + seg);
    } else if (q < 22) {
      const int seg = r - 1;
      if (seg < 0 || seg >= NSEG) continue;
      item_f1(ws, rs, smem, w, seg, q - 10);
      signal_done(ws, w, CNT_F1 + seg);
    } else {
      const int seg = r - 2;
      if (seg < 0 || seg >= NSEG) continue;
      item_f2(ws, rs, smem, a.out, w, seg, q - 22);
    }
  }
}

extern "C" void kernel_launch(void* const* d_in, const int* in_sizes, int n_in, void* d_out, int out_size, void* d_ws,
                              size_t ws_size, hipStream_t stream) {
  unsigned char* ws = (unsigned char*)d_ws;
  PrepArgs pa;
  pa.x = (const u16*)d_in[0]; pa.mem0 = (const u16*)d_in[1]; pa.wq = (const u16*)d_in[2]; pa.wk = (const u16*)d_in[3];
  pa.wv = (const u16*)d_in[4]; pa.wo = (const u16*)d_in[5]; pa.wg = (const u16*)d_in[8]; pa.wu = (const u16*)d_in[9];
  pa.wd = (const u16*)d_in[10]; pa.ws = ws;
  constexpr int LDS_MAIN = 136 * 1024;
  (void)hipFuncSetAttribute((const void*)k_main, hipFuncAttributeMaxDynamicSharedMemorySize, LDS_MAIN);
  k_prep<<<PREP_BLOCKS, 256, 0, stream>>>(pa);
  MainArgs ma;
  ma.x = pa.x; ma.wv = pa.wv; ma.ws = ws; ma.out = (u16*)d_out;
  ma.ct.dq0 = DQ0; ma.ct.dcm0 = DCM0; ma.ct.dcp0 = DCP0; ma.ct.dn = T_DN; ma.ct.up = T_UP; ma.ct.pad = 0;
  k_main<<<256, 256, LDS_MAIN, stream>>>(ma);
}
```
